```python
import torch

def _softmax(x, axis=-1):
    return torch.softmax(x, dim=axis)

def _gelu(x):
    return torch.nn.functional.gelu(x, approximate='tanh')

def _silu(x):
    return torch.nn.functional.silu(x)

def _sigmoid(x):
    return torch.sigmoid(x)

def _layernorm(x, axis=-1, eps=1e-05):
    m = x.mean(dim=axis, keepdim=True)
    v = x.var(dim=axis, keepdim=True, unbiased=False)
    return (x - m) / torch.sqrt(v + eps)

def _scatter_add(tgt, idx, src, axis=0):
    return tgt.clone().index_add_(axis, idx, src)

def _segment_sum(src, seg, n):
    out = torch.zeros((n,) + src.shape[1:], dtype=src.dtype)
    return out.index_add_(0, seg, src)

def _pad(x, pw):
    flat = tuple((v for pair in reversed(pw) for v in pair))
    return torch.nn.functional.pad(x, flat)
kernel_name = 'adaLN_cond__gqa_block_cached__dense_activation_relu__decode'

def setup_inputs():

    _w = lambda a: (lambda t: t.to(torch.bfloat16) if t.is_floating_point() else t)(torch.as_tensor(a))

    def _A_setup():
        B, Sq, D = (8, 1, 576)
        Hq, Hkv, Sk, Dh = (32, 16, 4096, 128)
        assert Hq % Hkv == 0, (Hq, Hkv)
        return {'x': _w((lambda *s: torch.randn(*s, dtype=torch.bfloat16) * 0.05)(B, Sq, D)), 'k_cache': _w((lambda *s: torch.randn(*s, dtype=torch.bfloat16) * 3.2)(B, Hkv, Sk, Dh)), 'v_cache': _w((lambda *s: torch.randn(*s, dtype=torch.bfloat16) * 0.4)(B, Hkv, Sk, Dh)), 'wq': _w((lambda *s: torch.randn(*s, dtype=torch.bfloat16) * 3.2)(D, Hq * Dh) * D ** (-0.5)), 'wo': _w((lambda *s: torch.randn(*s, dtype=torch.bfloat16) * 0.4)(Hq * Dh, D) * (Hq * Dh) ** (-0.5))}
    _A_in = _A_setup()

    def _B_setup():
        B, S, D, Dff = (8, 1, 576, 4608)
        return {'x': _w((lambda *s: torch.randn(*s, dtype=torch.bfloat16) * 0.05)(B, S, D)), 'w1': _w((lambda *s: torch.randn(*s, dtype=torch.bfloat16) * 0.05)(D, Dff)), 'w2': _w((lambda *s: torch.randn(*s, dtype=torch.bfloat16) * 0.05)(Dff, D))}
    _B_in = _B_setup()
    return {'A_x': _A_in['x'], 'A_k_cache': _A_in['k_cache'], 'A_v_cache': _A_in['v_cache'], 'A_wq': _A_in['wq'], 'A_wo': _A_in['wo'], 'B_x': _B_in['x'], 'B_w1': _B_in['w1'], 'B_w2': _B_in['w2'], 'c': _w((lambda *s: torch.randn(*s, dtype=torch.bfloat16) * 0.2)(8, 576) * 8.0), 'w_mod': _w((lambda *s: torch.randn(*s, dtype=torch.bfloat16) * 0.2)(576, 6 * 576) * 576 ** (-0.5) * 8.0)}

def reference(A_x, A_k_cache, A_v_cache, A_wq, A_wo, B_x, B_w1, B_w2, c, w_mod):

    def _A_ref(x, k_cache, v_cache, wq, wo):
        B, Sq, D = x.shape
        Hq, Dh = (32, 128)
        Hkv = k_cache.shape[1]
        rep = Hq // Hkv
        q = (lambda x, *d: x.permute(*d))((x @ wq).reshape(B, Sq, Hq, Dh), 0, 2, 1, 3)
        k = (lambda x, n, axis: torch.repeat_interleave(x, n, dim=axis))(k_cache, rep, axis=1)
        v = (lambda x, n, axis: torch.repeat_interleave(x, n, dim=axis))(v_cache, rep, axis=1)
        scores = torch.einsum('bhqd,bhkd->bhqk', q, k) * Dh ** (-0.5)
        p = _softmax(scores, axis=-1)
        o = torch.einsum('bhqk,bhkd->bhqd', p, v)
        return (lambda x, *d: x.permute(*d))(o, 0, 2, 1, 3).reshape(B, Sq, Hq * Dh) @ wo

    def _B_ref(x, w1, w2):
        h = torch.einsum('bsd,df->bsf', x, w1)
        zero = torch.zeros_like(h)
        h = torch.maximum(h, zero)
        return torch.einsum('bsf,fd->bsd', h, w2)
    x = A_x
    mod = (_silu(c) @ w_mod).reshape(c.shape[0], 6, 1, -1)
    sa, ca, ga, sb, cb, gb = (mod[:, 0].clone(), mod[:, 1].clone(), mod[:, 2].clone(), mod[:, 3].clone(), mod[:, 4].clone(), mod[:, 5].clone())
    h = _layernorm(x, axis=-1) * (1 + ca) + sa
    x = x + ga * _A_ref(h, A_k_cache, A_v_cache, A_wq, A_wo)
    h = _layernorm(x, axis=-1) * (1 + cb) + sb
    x = x + gb * _B_ref(h, B_w1, B_w2)
    return x
```

```cpp
#include <hip/hip_runtime.h>
#include <hip/hip_bf16.h>
#include <stdint.h>
typedef unsigned short u16;
typedef unsigned long long u64;
typedef short s16x4 __attribute__((ext_vector_type(4)));
typedef __bf16 bf16x8 __attribute__((ext_vector_type(8)));
typedef float f32x4 __attribute__((ext_vector_type(4)));
typedef unsigned u32x4 __attribute__((ext_vector_type(4)));
typedef unsigned u32x2 __attribute__((ext_vector_type(2)));
#define LAS __attribute__((address_space(3)))
#define MIMIC 1
__device__ __forceinline__ float bf2f(u16 v) { return __uint_as_float((unsigned)v << 16); }
__device__ __forceinline__ u16 f2bf(float f) { __bf16 h = (__bf16)f; return __builtin_bit_cast(u16, h); }
__device__ __forceinline__ float rbf(float f) { return bf2f(f2bf(f)); }
__device__ __forceinline__ unsigned pk2(float lo, float hi) { return (unsigned)f2bf(lo) | ((unsigned)f2bf(hi) << 16); }
template <int CTRL> __device__ __forceinline__ unsigned dppu(unsigned x) { return (unsigned)__builtin_amdgcn_mov_dpp((int)x, CTRL, 0xf, 0xf, true); }
template <int CTRL> __device__ __forceinline__ float dppf(float x) { return __uint_as_float(dppu<CTRL>(__float_as_uint(x))); }

__global__ void k_mod(const u16* c, const u16* wm, float* mod) {
  __shared__ float sc[8][576];
  int t = threadIdx.x;
  for (int i = t; i < 8*576; i += 512) { float v = bf2f(c[i]); sc[i/576][i%576] = rbf(v / (1.f + __expf(-v))); }
  __syncthreads();
  int col = blockIdx.x*64 + (t & 63), b = t >> 6; float a = 0;
  for (int k = 0; k < 576; k++) a += sc[b][k] * bf2f(wm[k*3456 + col]);
  mod[b*3456 + col] = rbf(a);
}
__device__ void ln_mod(const float* xin  , float (*h)[576], const float* mod, int so, int co) {
  int t = threadIdx.x, b = t >> 6, l = t & 63;
  float s = 0; for (int k = l; k < 576; k += 64) s += xin[b*576 + k];
  for (int o = 32; o; o >>= 1) s += __shfl_xor(s, o);
  float mean = s / 576.f, v = 0;
  for (int k = l; k < 576; k += 64) { float d = xin[b*576+k] - mean; v += d*d; }
  for (int o = 32; o; o >>= 1) v += __shfl_xor(v, o);
  float mb = rbf(mean), vb = rbf(v / 576.f), sd = rbf(sqrtf(rbf(vb + 1e-5f)));
  for (int k = l; k < 576; k += 64) { float n = rbf(rbf(xin[b*576+k] - mb) / sd); h[b][k] = rbf(rbf(n * rbf(1.f + mod[b*3456 + co + k])) + mod[b*3456 + so + k]); }
}
__global__ void k_q(const u16* x, const float* mod, const u16* wq, u16* q) {
  __shared__ float xs[8*576], h[8][576];
  int t = threadIdx.x;
  for (int i = t; i < 8*576; i += 512) xs[i] = bf2f(x[i]);
  __syncthreads(); ln_mod(xs, h, mod, 0, 576); __syncthreads();
  int col = blockIdx.x*64 + (t & 63), b = t >> 6; float a = 0;
  for (int k = 0; k < 576; k++) a += h[b][k] * bf2f(wq[k*4096 + col]);
  q[b*4096 + col] = f2bf(a);
}
__global__ void k_wo(const float* po, const float* pml, const u16* wo, const u16* x, const float* mod, float* x1) {
  __shared__ float o[4096]; __shared__ float red[8][64];
  int t = threadIdx.x, b = blockIdx.y;
  for (int i = t; i < 4096; i += 512) { int hq = i >> 7, d = i & 127, kvh = hq >> 1, h = hq & 1; float M = -INFINITY;
    const float* ml = pml + (size_t)((b*16 + kvh)*8) * 4;
    for (int p = 0; p < 8; p++) M = fmaxf(M, ml[p*4 + h*2]);
    float L = 0, a = 0; for (int p = 0; p < 8; p++) { float w = exp2f(ml[p*4 + h*2] - M); L += w * ml[p*4 + h*2 + 1]; a += w * po[(size_t)((b*16+kvh)*8 + p)*256 + h*128 + d]; }
    o[i] = rbf(a / L); }
  __syncthreads();
  int col = blockIdx.x*64 + (t & 63), ks = t >> 6; float a = 0;
  for (int k = ks*512; k < ks*512+512; k++) a += o[k] * bf2f(wo[k*576 + col]);
  red[ks][t & 63] = a; __syncthreads();
  if (t < 64) { float s = 0; for (int i = 0; i < 8; i++) s += red[i][t]; x1[b*576 + col] = rbf(bf2f(x[b*576+col]) + rbf(mod[b*3456 + 1152 + col] * rbf(s))); }
}
__global__ void k_w1(const float* x1, const float* mod, const u16* w1, float* h1) {
  __shared__ float xs[8*576], h[8][576];
  int t = threadIdx.x;
  for (int i = t; i < 8*576; i += 512) xs[i] = x1[i];
  __syncthreads(); ln_mod(xs, h, mod, 1728, 2304); __syncthreads();
  int col = blockIdx.x*64 + (t & 63), b = t >> 6; float a = 0;
  for (int k = 0; k < 576; k++) a += h[b][k] * bf2f(w1[k*4608 + col]);
  h1[b*4608 + col] = fmaxf(rbf(a), 0.f);
}
__global__ void k_w2(const float* h1, const u16* w2, const float* x1, const float* mod, u16* out) {
  __shared__ float red[8][64];
  int t = threadIdx.x, b = blockIdx.y;
  int col = blockIdx.x*64 + (t & 63), ks = t >> 6; float a = 0;
  for (int k = ks*576; k < ks*576+576; k++) a += h1[b*4608 + k] * bf2f(w2[k*576 + col]);
  red[ks][t & 63] = a; __syncthreads();
  if (t < 64) { float s = 0; for (int i = 0; i < 8; i++) s += red[i][t]; out[b*576 + col] = f2bf(x1[b*576+col] + rbf(mod[b*3456 + 2880 + col] * rbf(s))); }
}
__device__ __forceinline__ void dma4(const char* sbase, unsigned o0, unsigned o1, unsigned o2, unsigned o3, unsigned ldsdst) {
  unsigned keep;
  asm volatile("s_mov_b32 %0, m0\n\ts_mov_b32 m0, %6\n\ts_nop 0\n\t"
     "global_load_lds_dwordx4 %1, %5 offset:0 nt\n\tglobal_load_lds_dwordx4 %2, %5 offset:1024 nt\n\t"
     "global_load_lds_dwordx4 %3, %5 offset:2048 nt\n\tglobal_load_lds_dwordx4 %4, %5 offset:3072 nt\n\ts_mov_b32 m0, %0"
     : "=&s"(keep) : "v"(o0), "v"(o1), "v"(o2), "v"(o3), "s"(sbase), "s"(ldsdst) : "memory");
}
template<int N> __device__ __forceinline__ void waitvm() { asm volatile("s_waitcnt vmcnt(%0)" :: "i"(N) : "memory"); }
template<int R> __device__ __forceinline__ unsigned ror(unsigned x) { if constexpr (R == 0) return x; else return dppu<0x120 + R>(x); }
template<int T8, int U> __device__ __forceinline__ void pv1(f32x4& acc, unsigned z0, unsigned z1, s16x4 v) {
  constexpr int R = (2*(U - T8) + 16) & 15;
  u32x2 bb = {ror<R>(z0), ror<R>(z1)};
  acc = __builtin_amdgcn_mfma_f32_16x16x16bf16_1k(v, __builtin_bit_cast(s16x4, bb), acc, 0, 0, 0);
}
template<int T8> __device__ __forceinline__ void pv_tile(f32x4& acc, unsigned p0, unsigned p1, const s16x4 (&v)[8], int n2) {
  unsigned z0 = (n2 == T8) ? p0 : 0u, z1 = (n2 == T8) ? p1 : 0u;
  pv1<T8,0>(acc, z0, z1, v[0]); pv1<T8,1>(acc, z0, z1, v[1]); pv1<T8,2>(acc, z0, z1, v[2]); pv1<T8,3>(acc, z0, z1, v[3]);
  pv1<T8,4>(acc, z0, z1, v[4]); pv1<T8,5>(acc, z0, z1, v[5]); pv1<T8,6>(acc, z0, z1, v[6]); pv1<T8,7>(acc, z0, z1, v[7]);
}
__global__ void __launch_bounds__(256, 1) k_attn(const char* K, const char* V, const u16* q, float* po, float* pml) {
  extern __shared__ __attribute__((aligned(16))) char lds[];
  const int lane = threadIdx.x & 63, w = __builtin_amdgcn_readfirstlane(threadIdx.x >> 6);
  const int bid = blockIdx.x, xcd = bid & 7, idx = bid >> 3;
  const int kvh = 2*xcd + (idx & 1), b = (idx >> 1) & 7, half = idx >> 4;
  const size_t base = (size_t)(b*16 + kvh) * 1048576 + (size_t)half * 524288 + w * 4096;
  const char* kb = K + base; const char* vb = V + base;
  const int n = lane & 15, g = lane >> 4;
  unsigned ko[4], vo[4];
#pragma unroll
  for (int i = 0; i < 4; i++) { int row = 4*i + g; ko[i] = g * 256 + ((n ^ row) * 16); vo[i] = g * 256 + ((n ^ ((row & 7) << 1)) * 16); }
  bf16x8 qf[4];
#pragma unroll
  for (int s = 0; s < 4; s++) qf[s] = *(const bf16x8*)(q + b*4096 + (kvh*2 + (n & 1))*128 + (4*s + g)*8);
  asm volatile("s_waitcnt vmcnt(0)" : "+v"(qf[0]), "+v"(qf[1]), "+v"(qf[2]), "+v"(qf[3]) :: "memory");
  LAS char* ring = (LAS char*)lds + w * 16384;
  const unsigned ring0 = (unsigned)(size_t)ring;
  unsigned kr[4];
#pragma unroll
  for (int s = 0; s < 4; s++) kr[s] = n * 256 + (((4*s + g) ^ n) * 16);
  const int vrow = 4*g + (n >> 2), vp = n & 3;
  const unsigned vr0 = vrow * 256 + (vp >> 1) * 16 + (vp & 1) * 8, vx = (vrow & 7) << 5;
  dma4(kb, ko[0], ko[1], ko[2], ko[3], ring0);
  dma4(kb + 16384, ko[0], ko[1], ko[2], ko[3], ring0 + 4096);
  f32x4 S[4];
  const int n2 = n >> 1;
#pragma unroll
  for (int G = 0; G < 4; G++) {
#pragma unroll
    for (int t8 = 0; t8 < 8; t8++) {
      const int t = G*8 + t8;
      waitvm<4>();
      if (t + 2 < 32) dma4(kb + (t+2)*16384, ko[0], ko[1], ko[2], ko[3], ring0 + ((t+2)&3)*4096);
      else dma4(vb + (t+2-32)*16384, vo[0], vo[1], vo[2], vo[3], ring0 + ((t+2)&3)*4096);
      f32x4 d = {0,0,0,0};
#pragma unroll
      for (int s = 0; s < 4; s++) { bf16x8 kf = *(LAS bf16x8*)(ring + (t&3)*4096 + kr[s]); d = __builtin_amdgcn_mfma_f32_16x16x32_bf16(kf, qf[s], d, 0, 0, 0); }
#pragma unroll
      for (int r = 0; r < 4; r++) S[G][r] = (t8 == 0 || n2 == t8) ? d[r] : S[G][r];
    }
  }
  float m = -INFINITY;
#pragma unroll
  for (int G = 0; G < 4; G++)
#pragma unroll
    for (int r = 0; r < 4; r++) { float s = MIMIC ? rbf(rbf(S[G][r]) * 0.08838834764831845f) : S[G][r] * 0.08838834764831845f; s *= 1.4426950408889634f; S[G][r] = s; m = fmaxf(m, s); }
  m = fmaxf(m, dppf<0x4E>(m)); m = fmaxf(m, dppf<0x124>(m)); m = fmaxf(m, dppf<0x128>(m));
  m = fmaxf(m, __shfl_xor(m, 16)); m = fmaxf(m, __shfl_xor(m, 32));
  float l = 0; unsigned P[4][2];
#pragma unroll
  for (int G = 0; G < 4; G++) { float e[4];
#pragma unroll
    for (int r = 0; r < 4; r++) { e[r] = __builtin_amdgcn_exp2f(S[G][r] - m); l += e[r]; }
    P[G][0] = pk2(e[0], e[1]); P[G][1] = pk2(e[2], e[3]); }
  l += dppf<0x4E>(l); l += dppf<0x124>(l); l += dppf<0x128>(l); l += __shfl_xor(l, 16); l += __shfl_xor(l, 32);
  f32x4 acc = {0,0,0,0};
#define VT(T8) { const int t = G*8 + T8; if (t + 1 < 32) waitvm<4>(); else waitvm<0>(); \
      if (t + 2 < 32) dma4(vb + (t+2)*16384, vo[0], vo[1], vo[2], vo[3], ring0 + ((t+2)&3)*4096); \
      s16x4 v[8]; _Pragma("unroll") for (int u = 0; u < 8; u++) v[u] = __builtin_amdgcn_ds_read_tr16_b64_v4i16((LAS s16x4*)(ring + (t&3)*4096 + vr0 + ((u << 5) ^ vx))); \
      pv_tile<T8>(acc, P[G][0], P[G][1], v, n2); }
#pragma unroll
  for (int G = 0; G < 4; G++) { VT(0) VT(1) VT(2) VT(3) VT(4) VT(5) VT(6) VT(7) }
  const int p = (b*16 + kvh)*8 + half*4 + w;
  *(f32x4*)(po + (size_t)p*256 + (n & 1)*128 + n2*16 + g*4) = acc;
  if (lane < 2) { pml[p*4 + lane*2] = m; pml[p*4 + lane*2 + 1] = l; }
}
extern "C" void kernel_launch(void* const* d_in, const int* in_sizes, int n_in, void* d_out, int out_size, void* d_ws, size_t ws_size, hipStream_t stream) {
  const u16* x = (const u16*)d_in[0]; const char* K = (const char*)d_in[1]; const char* V = (const char*)d_in[2];
  const u16* wq = (const u16*)d_in[3]; const u16* wo = (const u16*)d_in[4]; const u16* w1 = (const u16*)d_in[6]; const u16* w2 = (const u16*)d_in[7];
  const u16* c = (const u16*)d_in[8]; const u16* wm = (const u16*)d_in[9];
  char* ws = (char*)d_ws;
  float* mod = (float*)ws; u16* q = (u16*)(ws + (1<<20)); float* po = (float*)(ws + (2<<20)); float* pml = (float*)(ws + (4<<20));
  float* x1 = (float*)(ws + (5<<20)); float* h1 = (float*)(ws + (6<<20));
#define REPS 1
  k_mod<<<54, 512, 0, stream>>>(c, wm, mod);
  k_q<<<64, 512, 0, stream>>>(x, mod, wq, q);
  for (int i = 0; i < REPS; i++) k_attn<<<256, 256, 65536, stream>>>(K, V, q, po, pml);
  k_wo<<<dim3(9,8), 512, 0, stream>>>(po, pml, wo, x, mod, x1);
  k_w1<<<72, 512, 0, stream>>>(x1, mod, w1, h1);
  k_w2<<<dim3(9,8), 512, 0, stream>>>(h1, w2, x1, mod, (u16*)d_out);
}
```

```cpp
#include <hip/hip_runtime.h>
#include <hip/hip_bf16.h>
#include <stdint.h>
typedef unsigned short u16;
typedef unsigned long long u64;
typedef short s16x4 __attribute__((ext_vector_type(4)));
typedef __bf16 bf16x8 __attribute__((ext_vector_type(8)));
typedef __bf16 bf16x2 __attribute__((ext_vector_type(2)));
typedef float f32x4 __attribute__((ext_vector_type(4)));
typedef float f32x2 __attribute__((ext_vector_type(2)));
typedef unsigned u32x4 __attribute__((ext_vector_type(4)));
typedef unsigned u32x2 __attribute__((ext_vector_type(2)));
#define LAS __attribute__((address_space(3)))
#define DEVI __device__ __forceinline__
#define STAMPS 0

constexpr int WS_EPOCH = 0;
constexpr int WS_FH = 1024, WS_FQ = 2048, WS_FP = 4096;
constexpr int WS_HG = 8192, HG_REP = 18 * 2048;
constexpr int WS_QG = WS_HG + 8 * HG_REP, QG_PL = 8 * 4096 * 4;
constexpr int WS_MODR = WS_QG + 6 * QG_PL;
constexpr int WS_ML = WS_MODR + 8 * 2304 * 4;
constexpr int WS_P1 = WS_ML + 256 * 128;
constexpr int WS_X1 = WS_P1 + 8 * 32 * 576 * 4, X1_REP = 8 * 576 * 4;
constexpr int WS_P2 = WS_X1 + 8 * X1_REP;
constexpr int WS_END = WS_P2 + 8 * 96 * 576 * 4;
constexpr int WS_STAMP = 4 << 20;
constexpr int L_W2 = 49152, L_W1 = L_W2 + 55296;
constexpr int L_STG = 131072, L_RED = 131072, L_NX = L_RED + 17408, L_XT = 98304;
constexpr int L_MISC = 159744;
constexpr int L_CNT = L_MISC, L_ML = L_MISC + 64, L_SC = L_MISC + 128, L_Q = L_MISC + 256, L_H1 = L_MISC + 1024, L_TP = L_MISC + 2048;
constexpr int H2_STR = 1216, H1_STR = 144;
constexpr int L_TOTAL = 163840;
constexpr unsigned SPIN_CAP = 400000;

DEVI float bf2f(unsigned v) { return __uint_as_float(v << 16); }
DEVI float hi2f(unsigned v) { return __uint_as_float(v & 0xffff0000u); }
DEVI unsigned pk2(float lo, float hi) { bf16x2 v = {(__bf16)lo, (__bf16)hi}; return __builtin_bit_cast(unsigned, v); }
DEVI float rbf(float f) { return hi2f(pk2(f, f)); }
template <int CTRL> DEVI unsigned dppu(unsigned x) { return (unsigned)__builtin_amdgcn_mov_dpp((int)x, CTRL, 0xf, 0xf, true); }
template <int CTRL> DEVI float dppf(float x) { return __uint_as_float(dppu<CTRL>(__float_as_uint(x))); }
constexpr int XOR1 = 0xB1, XOR2 = 0x4E, HMIR = 0x141, ROR4 = 0x124, ROR8 = 0x128;
DEVI float x16sum(float x) { auto s = __builtin_amdgcn_permlane16_swap(__float_as_uint(x), __float_as_uint(x), false, false); return __uint_as_float(s[0]) + __uint_as_float(s[1]); }
DEVI float x32sum(float x) { auto s = __builtin_amdgcn_permlane32_swap(__float_as_uint(x), __float_as_uint(x), false, false); return __uint_as_float(s[0]) + __uint_as_float(s[1]); }
DEVI float x16max(float x) { auto s = __builtin_amdgcn_permlane16_swap(__float_as_uint(x), __float_as_uint(x), false, false); return fmaxf(__uint_as_float(s[0]), __uint_as_float(s[1])); }
DEVI float x32max(float x) { auto s = __builtin_amdgcn_permlane32_swap(__float_as_uint(x), __float_as_uint(x), false, false); return fmaxf(__uint_as_float(s[0]), __uint_as_float(s[1])); }
DEVI unsigned lo2(unsigned a, unsigned b) { return __builtin_amdgcn_perm(b, a, 0x05040100u); }
DEVI unsigned hi2(unsigned a, unsigned b) { return __builtin_amdgcn_perm(b, a, 0x07060302u); }
DEVI bf16x8 asbf8(u32x4 v) { return __builtin_bit_cast(bf16x8, v); }
DEVI f32x4 mfma32(bf16x8 a, bf16x8 b, f32x4 c) { return __builtin_amdgcn_mfma_f32_16x16x32_bf16(a, b, c, 0, 0, 0); }
DEVI f32x4 mfma16(u32x2 a, u32x2 b, f32x4 c) { return __builtin_amdgcn_mfma_f32_16x16x16bf16_1k(__builtin_bit_cast(s16x4, a), __builtin_bit_cast(s16x4, b), c, 0, 0, 0); }
DEVI u32x2 trread(LAS char* p) { return __builtin_bit_cast(u32x2, __builtin_amdgcn_ds_read_tr16_b64_v4i16((LAS s16x4*)p)); }
DEVI void cfence() { asm volatile("" ::: "memory"); }
DEVI void wg_barrier() { asm volatile("s_waitcnt lgkmcnt(0)\n\ts_barrier" ::: "memory"); }
template <int N> DEVI void waitvm() { asm volatile("s_waitcnt vmcnt(%0)" ::"i"(N) : "memory"); }
DEVI void waitlgkm() { asm volatile("s_waitcnt lgkmcnt(0)" ::: "memory"); }
typedef __amdgpu_buffer_rsrc_t rsrc_t;
DEVI u32x4 ld128(rsrc_t rs, int off) { return __builtin_amdgcn_raw_buffer_load_b128(rs, off, 0, 16); }
DEVI void st128(rsrc_t rs, int off, u32x4 v) { __builtin_amdgcn_raw_buffer_store_b128(v, rs, off, 0, 16); }
DEVI rsrc_t mkrs(const char* p, int bytes) { return __builtin_amdgcn_make_buffer_rsrc((void*)p, 0, bytes, 0x00020000); }
template <int BANK> DEVI float banksel(float old, float v) { return __uint_as_float((unsigned)__builtin_amdgcn_update_dpp((int)__float_as_uint(old), (int)__float_as_uint(v), 0xE4, 0xf, 1 << BANK, false)); }
DEVI float sel4(float a, float b, float c, float d) { return banksel<3>(banksel<2>(banksel<1>(a, b), c), d); }
template <class T> DEVI void pin(T& v) { asm volatile("" : "+v"(v)); }
DEVI unsigned ld32(rsrc_t rs, int off) { return __builtin_amdgcn_raw_buffer_load_b32(rs, off, 0, 16); }
DEVI void st32(rsrc_t rs, int off, unsigned v) { __builtin_amdgcn_raw_buffer_store_b32(v, rs, off, 0, 16); }
DEVI unsigned xor4(u32x4 v, unsigned t) { return (v[0] ^ t) | (v[1] ^ t) | (v[2] ^ t) | (v[3] ^ t); }
DEVI bool all_ok(unsigned bad) { return __builtin_amdgcn_ballot_w64(bad != 0) == 0; }
DEVI unsigned tag16(float v, unsigned t) { return (pk2(v, v) & 0xffff0000u) | (t & 0xffffu); }
DEVI unsigned tag10(float v, unsigned t10) { return (__float_as_uint(v) & ~0x3ffu) | t10; }
DEVI float un10(unsigned v) { return __uint_as_float(v & ~0x3ffu); }
DEVI void hint_wait(rsrc_t rs, int off, unsigned E) {
  _Pragma("unroll 1") for (unsigned s = 0; s < SPIN_CAP; s++) { cfence(); if (all_ok(ld32(rs, off) ^ E)) break; }
  cfence();
}
DEVI void lds_arrive(LAS char* L, int i) { waitlgkm(); if ((threadIdx.x & 63) == 0) __hip_atomic_fetch_add((LAS unsigned*)(L + L_CNT + 4 * i), 1u, __ATOMIC_RELAXED, __HIP_MEMORY_SCOPE_WORKGROUP); cfence(); }
template <int SLEEP> DEVI void lds_wait(LAS char* L, int i, unsigned target) {
  _Pragma("unroll 1") for (unsigned s = 0; s < 16 * SPIN_CAP; s++) {
    unsigned v = *(volatile LAS unsigned*)(L + L_CNT + 4 * i);
    if (__builtin_amdgcn_readfirstlane(v) >= target) break;
    if (SLEEP) __builtin_amdgcn_s_sleep(SLEEP);
  }
  cfence();
}
#define DMA4_BODY(POL) asm volatile("s_mov_b32 %0, m0\n\ts_mov_b32 m0, %6\n\ts_nop 0\n\t" \
     "global_load_lds_dwordx4 %1, %5 offset:0" POL "\n\tglobal_load_lds_dwordx4 %2, %5 offset:1024" POL "\n\t" \
     "global_load_lds_dwordx4 %3, %5 offset:2048" POL "\n\tglobal_load_lds_dwordx4 %4, %5 offset:3072" POL "\n\ts_mov_b32 m0, %0" \
     : "=&s"(keep) : "v"(o[0]), "v"(o[1]), "v"(o[2]), "v"(o[3]), "s"(sbase), "s"(dst) : "memory")
template <int NT> DEVI void dma4(const char* sbase, const unsigned (&o)[4], unsigned dst) { unsigned keep; if (NT) DMA4_BODY(" nt"); else DMA4_BODY(""); }
template <int NT> DEVI void dma1(const char* sbase, unsigned o, unsigned dst) {
  unsigned keep;
  if (NT) asm volatile("s_mov_b32 %0, m0\n\ts_mov_b32 m0, %3\n\ts_nop 0\n\tglobal_load_lds_dwordx4 %1, %2 nt\n\ts_mov_b32 m0, %0" : "=&s"(keep) : "v"(o), "s"(sbase), "s"(dst) : "memory");
  else asm volatile("s_mov_b32 %0, m0\n\ts_mov_b32 m0, %3\n\ts_nop 0\n\tglobal_load_lds_dwordx4 %1, %2\n\ts_mov_b32 m0, %0" : "=&s"(keep) : "v"(o), "s"(sbase), "s"(dst) : "memory");
}
#define WNT 0
template <int R> DEVI unsigned ror(unsigned x) { if constexpr (R == 0) return x; else return dppu<0x120 + R>(x); }
template <int T8, int U> DEVI void pv1(f32x4& acc, unsigned z0, unsigned z1, u32x2 v) {
  constexpr int R = (2 * (U - T8) + 16) & 15;
  acc = mfma16(v, u32x2{ror<R>(z0), ror<R>(z1)}, acc);
}
#define NPRE 8
constexpr int NF = 8 + NPRE;
constexpr int slot_of(int t) { return t < NF ? t % 8 : (t - NF) % 3; }
#define W0 28
constexpr bool wiss(int t) { return t >= W0 && t < W0 + 32; }
template <int WT> DEVI void dma4_paced(const char* sbase, const unsigned (&o)[4], unsigned dst) { unsigned keep;
  asm volatile("s_mov_b32 %0, m0\n\ts_mov_b32 m0, %6\n\ts_waitcnt vmcnt(%7) lgkmcnt(0)\n\t"
     "global_load_lds_dwordx4 %1, %5 offset:0 nt\n\ts_waitcnt vmcnt(%7)\n\tglobal_load_lds_dwordx4 %2, %5 offset:1024 nt\n\ts_waitcnt vmcnt(%7)\n\t"
     "global_load_lds_dwordx4 %3, %5 offset:2048 nt\n\ts_waitcnt vmcnt(%7)\n\tglobal_load_lds_dwordx4 %4, %5 offset:3072 nt\n\ts_mov_b32 m0, %0"
     : "=&s"(keep) : "v"(o[0]), "v"(o[1]), "v"(o[2]), "v"(o[3]), "s"(sbase), "s"(dst), "i"(WT) : "memory"); }
#define W1W 1
#define W2W 14
#define DLY_B 2
#define DLY_C 12
#define WPACE 4
#define DLY_Q 8
#define PW 4
constexpr int OSLOT = slot_of(62), L_H2 = slot_of(63) * 16384;
static_assert(NPRE >= 4 && NPRE <= 8, "schedule");
template <int I, int N, class F> DEVI void sfor(F&& f) { if constexpr (I < N) { f(std::integral_constant<int, I>{}); sfor<I + 1, N>(f); } }
#define STAMP(i)

__global__ void __launch_bounds__(512, 2) fused_block(const char* Kc, const char* Vc, const char* x, const char* wq, const char* wo, const char* w1,
                                                      const char* w2, const char* cc, const char* wm, char* out, char* ws) {
  __shared__ __attribute__((aligned(16))) char lds_mem[L_TOTAL];
  LAS char* L = (LAS char*)lds_mem;
  const unsigned L0 = (unsigned)(size_t)L;
  const int lane = threadIdx.x & 63, w = __builtin_amdgcn_readfirstlane(threadIdx.x >> 6);
  const int bid = blockIdx.x, xcd = bid & 7, idx = bid >> 3;
  const int kvh = 2 * xcd + (idx & 1), b = (idx >> 1) & 7, half = idx >> 4;
  const int n = lane & 15, g = lane >> 4;
  const rsrc_t rs = __builtin_amdgcn_make_buffer_rsrc((void*)ws, 0, WS_END, 0x00020000);
  STAMP(0);
  if (threadIdx.x < 12) *(LAS unsigned*)(L + L_CNT + threadIdx.x * 4) = 0;
  wg_barrier();
  u32x4 wof[4][8];
  u32x4 wox[8];
  unsigned E;
  unsigned g1p[6], sbp[6];
  const int i12 = idx % 12, grp = idx / 12, slice = 12 * xcd + i12;
  const bool task = idx < 24 && i12 < 9;
  const int tb = 4 * grp + (xcd >> 1), tcol = 32 * (9 * (xcd & 1) + i12) + 4 * (lane & 7), p8 = lane >> 3;
  const unsigned wsrc = (lane >> 3) * 1152 + (lane & 7) * 16;
  const char* wob = wo + (size_t)kvh * 256 * 1152 + w * 128;
  const int j1 = (idx - 24) * 8 + xcd;
  const bool s1 = idx >= 24 && j1 < 54;
  const unsigned wrd = (8 * g + (n >> 2)) * 128 + (n & 3) * 8;

  if (w < 4) {
    const size_t base = (size_t)(b * 16 + kvh) * 1048576 + (size_t)half * 524288 + w * 4096;
    const char* kb = Kc + base; const char* vb = Vc + base;
    unsigned ko[4], vo[4];
#pragma unroll
    for (int i = 0; i < 4; i++) { const int row = 4 * i + g; ko[i] = g * 256 + ((n ^ row) * 16); vo[i] = g * 256 + ((n ^ ((row & 7) << 1)) * 16); }
    const unsigned ring0 = L0 + w * 4096;
    LAS char* ring = L + w * 4096;
    unsigned kr[4];
#pragma unroll
    for (int s = 0; s < 4; s++) kr[s] = n * 256 + (((4 * s + g) ^ n) * 16);
    auto issue = [&](auto T) { constexpr int t = T; waitlgkm();
      if constexpr (t < 32) dma4<1>(kb + t * 16384, ko, ring0 + slot_of(t) * 16384);
      else if constexpr (t < 64) dma4<1>(vb + (t - 32) * 16384, vo, ring0 + slot_of(t) * 16384); };
    bf16x8 pre[NPRE][4];
    if (s1) lds_wait<0>(L, 4, 4);
    sfor<0, NF>([&](auto T) { constexpr int t = T;
      if (s1) dma4_paced<7>(kb + t * 16384, ko, ring0 + slot_of(t) * 16384); else dma4_paced<PW>(kb + t * 16384, ko, ring0 + slot_of(t) * 16384);
      if constexpr (t >= 2 && t - 2 < NPRE) {
#pragma unroll
        for (int s = 0; s < 4; s++) pre[t - 2][s] = *(LAS bf16x8*)(ring + (t - 2) * 16384 + kr[s]); } });
    const int vrow = 4 * g + (n >> 2), vp = n & 3, n2 = n >> 1;
    const unsigned vr0 = vrow * 256 + (vp >> 1) * 16 + (vp & 1) * 8, vx = (vrow & 7) << 5;
    STAMP(1);
    lds_wait<0>(L, 8, 1);
    STAMP(2);
    bf16x8 qf[4];
    f32x4 S[4];
    auto score = [&](auto T, const bf16x8 (&kf)[4]) { constexpr int t = T;
      f32x4 d = {0, 0, 0, 0};
#pragma unroll
      for (int s = 0; s < 4; s++) d = mfma32(kf[s], qf[s], d);
#pragma unroll
      for (int r = 0; r < 4; r++) S[t >> 3][r] = ((t & 7) == 0 || n2 == (t & 7)) ? d[r] : S[t >> 3][r]; };
    auto kread = [&](auto T, bf16x8 (&kf)[4]) { constexpr int t = T;
#pragma unroll
      for (int s = 0; s < 4; s++) kf[s] = *(LAS bf16x8*)(ring + slot_of(t) * 16384 + kr[s]); };
    auto wstep = [&](auto T) { constexpr int t = T;
      if constexpr (t == 63) waitvm<0>(); else if constexpr (wiss(t - 1)) waitvm<5>(); else waitvm<4>();
      issue(std::integral_constant<int, t + 2>{});
      if constexpr (wiss(t)) dma1<WNT>(wob + (t - W0) * 8 * 1152, wsrc, ring0 + (3 + (((t - W0) >> 2) & 1)) * 16384 + ((t - W0) & 3) * 1024); };
    auto wuse = [&](auto T) { constexpr int t = T;
      if constexpr (wiss(t - 2) && ((t - 2 - W0) & 3) == 3) { constexpr int s = (t - 2 - W0) >> 2;
#pragma unroll
        for (int u = 0; u < 4; u++) { LAS char* p = ring + (3 + (s & 1)) * 16384 + wrd + u * 32; const u32x2 a = trread(p), c2 = trread(p + 512); wof[u][s] = u32x4{a[0], a[1], c2[0], c2[1]}; }
        if constexpr (s == 7) lds_arrive(L, 7); } };
    {
      bf16x8 k8[4], k9[4];
      kread(std::integral_constant<int, 8>{}, k8); issue(std::integral_constant<int, NF>{});
      kread(std::integral_constant<int, 9>{}, k9); issue(std::integral_constant<int, NF + 1>{});
#pragma unroll
      for (int s = 0; s < 4; s++) qf[s] = *(LAS bf16x8*)(L + L_Q + (n & 1) * 256 + (4 * s + g) * 16);
      E = *(LAS unsigned*)(L + L_CNT + 60);
      score(std::integral_constant<int, 8>{}, k8); score(std::integral_constant<int, 9>{}, k9);
    }
    sfor<0, NPRE>([&](auto T) { score(T, pre[T]); });
    sfor<NPRE, 32>([&](auto T) { constexpr int t = T;
      if constexpr (t != 8 && t != 9) {
        if constexpr (t == NF - 2) waitvm<12>(); else if constexpr (t == NF - 1) waitvm<8>(); else if constexpr (t >= NF) wstep(T);
        bf16x8 kf[4]; kread(T, kf); score(T, kf); wuse(T);
        if constexpr (t == NF) lds_arrive(L, 5);
      } });
    STAMP(3);
    float m = -INFINITY;
#pragma unroll
    for (int G = 0; G < 4; G++)
#pragma unroll
      for (int r = 0; r < 4; r++) { const float s = rbf(rbf(S[G][r]) * 0.08838834764831845f); S[G][r] = s; m = fmaxf(m, s); }
    m = fmaxf(m, dppf<XOR2>(m)); m = fmaxf(m, dppf<ROR4>(m)); m = fmaxf(m, dppf<ROR8>(m)); m = x16max(m); m = x32max(m);
    const float msub = (m == -INFINITY) ? 0.f : m;
    float l = 0; unsigned P[4][2];
#pragma unroll
    for (int G = 0; G < 4; G++) { float e[4];
#pragma unroll
      for (int r = 0; r < 4; r++) { e[r] = __builtin_amdgcn_exp2f((S[G][r] - msub) * 1.4426950408889634f); l += e[r]; }
      P[G][0] = pk2(e[0], e[1]); P[G][1] = pk2(e[2], e[3]); }
    l += dppf<XOR2>(l); l += dppf<ROR4>(l); l += dppf<ROR8>(l); l = x16sum(l); l = x32sum(l);
    if (lane < 2) *(LAS f32x2*)(L + L_ML + w * 16 + lane * 8) = f32x2{m, l};
    lds_arrive(L, 2);
    f32x4 acc = {0, 0, 0, 0};
    float sc; unsigned oaddr;
    sfor<32, 64>([&](auto T) { constexpr int t = T; constexpr int T8 = t & 7, G = (t - 32) >> 3;
      wstep(T);
      u32x2 v[8];
#pragma unroll
      for (int u = 0; u < 8; u++) v[u] = trread(ring + slot_of(t) * 16384 + vr0 + ((u << 5) ^ vx));
      const unsigned z0 = (n2 == T8) ? P[G][0] : 0u, z1 = (n2 == T8) ? P[G][1] : 0u;
      sfor<0, 8>([&](auto U) { pv1<T8, U>(acc, z0, z1, v[U]); });
      wuse(T);
      if constexpr (t == 56) { lds_wait<1>(L, 3, 1); sc = *(LAS float*)(L + L_SC + w * 8 + (n & 1) * 4);
        oaddr = w * (4096 + 64) + OSLOT * 16384 + ((n & 1) * 128 + n2 * 16 + g * 4) * 2; pin(sc); pin(oaddr); }
    });
    STAMP(4);
    *(LAS u32x2*)(L + oaddr) = u32x2{pk2(acc[0] * sc, acc[1] * sc), pk2(acc[2] * sc, acc[3] * sc)};
  } else {
    const int sw = w - 4, ks = n >> 3, c8 = n & 7;
    const unsigned Ev = __builtin_amdgcn_raw_buffer_load_b32(rs, WS_EPOCH, 0, 16);
    u32x4 wr[3][8];
    f32x4 a[8];
#pragma unroll
    for (int e = 0; e < 8; e++) a[e] = f32x4{0, 0, 0, 0};
    auto xpose = [&](u32x4 (&r)[8]) { u32x4 f[8];
#pragma unroll
      for (int e = 0; e < 4; e++)
#pragma unroll
        for (int j = 0; j < 4; j++) { f[2 * e][j] = lo2(r[2 * j][e], r[2 * j + 1][e]); f[2 * e + 1][j] = hi2(r[2 * j][e], r[2 * j + 1][e]); }
#pragma unroll
      for (int e = 0; e < 8; e++) r[e] = f[e]; };
    auto mm8 = [&](u32x4 af, const u32x4 (&f)[8]) {
#pragma unroll
      for (int e = 0; e < 8; e++) a[e] = mfma32(asbf8(af), asbf8(f[e]), a[e]); };
    auto redwrite = [&]() {
      if ((g >> 1) == ks) { LAS char* p = L + L_RED + ((sw * 2 + ks) * 8 + (g & 1) * 4) * 272 + c8 * 32;
#pragma unroll
        for (int r = 0; r < 4; r++) { *(LAS f32x4*)(p + r * 272) = f32x4{a[0][r], a[1][r], a[2][r], a[3][r]}; *(LAS f32x4*)(p + r * 272 + 16) = f32x4{a[4][r], a[5][r], a[6][r], a[7][r]}; } }
      lds_arrive(L, 0); };
    const int krow = 64 * sw + 32 * ks + 8 * g;
    if (s1) {
      const rsrc_t rc = mkrs(cc, 9216), rx = mkrs(x, 9216), rm = mkrs(wm, 576 * 6912);
      const int co = (c8 * 576 + krow) * 2, wo_ = krow * 6912 + j1 * 128 + c8 * 16;
      const int fc = 32 * ks + 8 * g + 4 * (sw & 1);
      const bool isca = j1 >= 9 && j1 < 18;
      const int fb = 2 * sw + (g & 1), j24 = (g >> 1) * 16 + n;
      u32x4 cv[3], xv[3];
      cv[0] = __builtin_amdgcn_raw_buffer_load_b128(rc, co, 0, 0); cv[1] = __builtin_amdgcn_raw_buffer_load_b128(rc, co + 512, 0, 0);
      cv[2] = __builtin_amdgcn_raw_buffer_load_b128(rc, co + 1024, 0, 0);
#pragma unroll
      for (int i = 0; i < 3; i++) xv[i] = __builtin_amdgcn_raw_buffer_load_b128(rx, (isca && j24 < 24) ? fb * 1152 + (j24 + 24 * i) * 16 : 9216, 0, 0);
      const u32x2 xs = __builtin_amdgcn_raw_buffer_load_b64(rx, isca ? fb * 1152 + (j1 - 9) * 128 + n * 8 : 9216, 0, 0);
#pragma unroll
      for (int i = 0; i < 2; i++)
#pragma unroll
        for (int j = 0; j < 8; j++) wr[i][j] = __builtin_amdgcn_raw_buffer_load_b128(rm, wo_, (i * 256 + j) * 6912, 0);
      if (sw == 0) {
#pragma unroll
        for (int j = 0; j < 8; j++) wr[2][j] = __builtin_amdgcn_raw_buffer_load_b128(rm, wo_, (512 + j) * 6912, 0);
      }
      if (j1 >= 18) lds_arrive(L, 4);
      cfence();
      STAMP(13);
      E = __builtin_amdgcn_readfirstlane(Ev) + 1u;
      if (sw == 0 && j1 < 18 && lane < 8) st32(rs, WS_FH + lane * 128 + j1 * 4, E);
#pragma unroll
      for (int i = 0; i < 3; i++)
#pragma unroll
        for (int j = 0; j < 4; j++) { const float v0 = bf2f(cv[i][j]), v1 = hi2f(cv[i][j]);
          cv[i][j] = pk2(v0 * __builtin_amdgcn_rcpf(1.f + __expf(-v0)), v1 * __builtin_amdgcn_rcpf(1.f + __expf(-v1))); }
      if (isca) {
        float s = 0;
#pragma unroll
        for (int i = 0; i < 12; i++) s += bf2f(xv[i >> 2][i & 3]) + hi2f(xv[i >> 2][i & 3]);
        s += dppf<XOR1>(s); s += dppf<XOR2>(s); s += dppf<ROR4>(s); s += dppf<ROR8>(s); s = x32sum(s);
        const float mean = s * (1.f / 576.f); float v = 0;
#pragma unroll
        for (int i = 0; i < 12; i++) { const float d0 = bf2f(xv[i >> 2][i & 3]) - mean, d1 = hi2f(xv[i >> 2][i & 3]) - mean; v += d0 * d0; v += d1 * d1; }
        if (j24 >= 24) v = 0;
        v += dppf<XOR1>(v); v += dppf<XOR2>(v); v += dppf<ROR4>(v); v += dppf<ROR8>(v); v = x32sum(v);
        const float mb = rbf(mean), sd = rbf(sqrtf(rbf(rbf(v * (1.f / 576.f)) + 1e-5f)));
        f32x4 nx;
#pragma unroll
        for (int r = 0; r < 4; r++) nx[r] = rbf(rbf(((r & 1) ? hi2f(xs[r >> 1]) : bf2f(xs[r >> 1])) - mb) / sd);
        *(LAS f32x4*)(L + L_NX + fb * 272 + n * 16) = nx;
      }
      cfence();
      xpose(wr[0]); mm8(cv[0], wr[0]); xpose(wr[1]); mm8(cv[1], wr[1]);
      if (sw == 0) { xpose(wr[2]); mm8(cv[2], wr[2]); }
      STAMP(1);
      redwrite();
      lds_wait<0>(L, 0, 4);
      const f32x4 nx = *(LAS f32x4*)(L + L_NX + c8 * 272 + fc * 4);
      f32x4 s = {0, 0, 0, 0};
#pragma unroll
      for (int p = 0; p < 8; p++) s += *(LAS f32x4*)(L + L_RED + (p * 8 + c8) * 272 + fc * 4);
      u32x4 y;
#pragma unroll
      for (int r = 0; r < 4; r++) y[r] = tag16(isca ? nx[r] * rbf(1.f + rbf(s[r])) : s[r], E);
      if (j1 < 18) { const int o = WS_HG + (sw >> 1) * 4 * HG_REP + j1 * 2048 + (sw & 1) * 1024 + lane * 16;
#pragma unroll
        for (int i = 0; i < 4; i++) st128(rs, o + i * HG_REP, y);
        lds_arrive(L, 4); }
      else if (sw < 2) st128(rs, WS_MODR + (c8 * 2304 + (j1 - 18) * 64 + fc) * 4, y);
      STAMP(2);
    } else if (idx < 24 && sw < 3) {
      const rsrc_t rq = mkrs(wq, 576 * 8192);
      const int bl = 24 * xcd + idx, k3 = bl >> 6, qc = 64 * (bl & 63), k0 = 192 * k3 + krow;
#pragma unroll
      for (int j = 0; j < 8; j++) wr[0][j] = __builtin_amdgcn_raw_buffer_load_b128(rq, k0 * 8192 + qc * 2 + c8 * 16, j * 8192, 0);
      cfence();
      STAMP(13);
      E = __builtin_amdgcn_readfirstlane(Ev) + 1u;
      xpose(wr[0]);
#pragma unroll
      for (int e = 0; e < 8; e++) pin(wr[0][e]);
      STAMP(1);
      const int hoff = WS_HG + xcd * HG_REP + (3 * k3 + sw) * 2048 + lane * 16;
      hint_wait(rs, WS_FH + xcd * 128 + (3 * k3 + sw + 9 * (lane & 1)) * 4, E);
      u32x4 h[4];
      _Pragma("unroll 1") for (unsigned s = 0; s < SPIN_CAP; s++) {
        cfence();
        h[0] = ld128(rs, hoff); h[1] = ld128(rs, hoff + 1024); h[2] = ld128(rs, hoff + 9 * 2048); h[3] = ld128(rs, hoff + 9 * 2048 + 1024);
        if (all_ok((xor4(h[0], E) | xor4(h[1], E) | xor4(h[2], E) | xor4(h[3], E)) & 0xffffu)) break;
      }
      cfence();
      STAMP(3);
      u32x4 af;
#pragma unroll
      for (int j = 0; j < 4; j++) af[j] = pk2(hi2f(h[j >> 1][(2 * j) & 3]) + hi2f(h[2 + (j >> 1)][(2 * j) & 3]), hi2f(h[j >> 1][(2 * j + 1) & 3]) + hi2f(h[2 + (j >> 1)][(2 * j + 1) & 3]));
      mm8(af, wr[0]);
      redwrite();
      lds_wait<0>(L, 0, 3);
      if (sw == 2) { if (lane == 0) st32(rs, WS_FQ + ((bl >> 3) & 7) * 128 + (k3 * 8 + (bl & 7)) * 4, E); }
      else {
        f32x4 s = {0, 0, 0, 0};
#pragma unroll
        for (int p = 0; p < 6; p++) s += *(LAS f32x4*)(L + L_RED + (p * 8 + 4 * sw + g) * 272 + n * 16);
        u32x4 yh, yl;
#pragma unroll
        for (int r = 0; r < 4; r++) { const unsigned u = __float_as_uint(s[r]); yh[r] = (u & 0xffff0000u) | (E & 0xffffu); yl[r] = (u << 16) | (E & 0xffffu); }
        const int o = WS_QG + 2 * k3 * QG_PL + ((4 * sw + g) * 4096 + qc + 4 * n) * 4;
        st128(rs, o, yh); st128(rs, o + QG_PL, yl);
      }
      STAMP(2);
    } else {
      if (idx < 24) {
        const rsrc_t rm = mkrs(wm, 576 * 6912);
        const int l24 = lane >= 24 ? 1 : 0; unsigned pf[4];
#pragma unroll
        for (int i = 0; i < 4; i++) { const int j = xcd + 8 * (2 * i + l24);
          pf[i] = __builtin_amdgcn_raw_buffer_load_b32(rm, (lane < 48 && j < 54) ? (24 * idx + lane - 24 * l24) * 6912 + j * 128 : 576 * 6912, 0, 0); }
        asm volatile("" :: "v"(pf[0]), "v"(pf[1]), "v"(pf[2]), "v"(pf[3]));
      }
      E = __builtin_amdgcn_readfirstlane(Ev) + 1u;
    }
    lds_arrive(L, 9);
    if (sw == 3) {
      u32x4 qv[6];
      if (idx < 24) { lds_wait<1>(L, 0, 3); __builtin_amdgcn_s_sleep(DLY_Q); }
      else hint_wait(rs, WS_FQ + xcd * 128 + ((g < 3 ? g : 2) * 8 + (idx & 1) * 4 + (lane & 3)) * 4, E);
      _Pragma("unroll 1") for (unsigned s = 0; s < SPIN_CAP; s++) {
        cfence(); unsigned bad = 0;
#pragma unroll
        for (int i = 0; i < 6; i++) qv[i] = ld128(rs, WS_QG + i * QG_PL + (b * 4096 + kvh * 256 + 4 * lane) * 4);
#pragma unroll
        for (int i = 0; i < 6; i++) bad |= xor4(qv[i], E);
        if (all_ok(bad & 0xffffu)) break;
      }
      cfence();
      float qs[4];
#pragma unroll
      for (int r = 0; r < 4; r++) qs[r] = (__uint_as_float(hi2(qv[1][r], qv[0][r])) + __uint_as_float(hi2(qv[3][r], qv[2][r]))) + __uint_as_float(hi2(qv[5][r], qv[4][r]));
      *(LAS u32x2*)(L + L_Q + lane * 8) = u32x2{pk2(qs[0], qs[1]), pk2(qs[2], qs[3])};
      if (lane == 0) *(LAS unsigned*)(L + L_CNT + 60) = E;
      lds_arrive(L, 8);
    }
    STAMP(4);
    lds_wait<2>(L, 9, 4);
    {
      const unsigned stg0 = L0 + L_STG + sw * 4096; LAS char* stg = L + L_STG + sw * 4096;
      sfor<0, 8>([&](auto S_) { constexpr int s = S_;
        waitlgkm();
        dma1<WNT>(wob + (4 * s) * 9216, wsrc, stg0); dma1<WNT>(wob + (4 * s + 1) * 9216, wsrc, stg0 + 1024);
        if (WPACE == 2) waitvm<0>();
        dma1<WNT>(wob + (4 * s + 2) * 9216, wsrc, stg0 + 2048);
        if (WPACE == 3) waitvm<2>();
        dma1<WNT>(wob + (4 * s + 3) * 9216, wsrc, stg0 + 3072); waitvm<0>();
#pragma unroll
        for (int u = 0; u < 4; u++) { LAS char* p = stg + wrd + u * 32; const u32x2 a = trread(p), c2 = trread(p + 512); wof[u][s] = u32x4{a[0], a[1], c2[0], c2[1]}; }
      });
      lds_wait<8>(L, 5, 4);
      const char* xb = wo + (size_t)kvh * 256 * 1152 + 1024 + sw * 8 * 9216;
      _Pragma("unroll 1") for (int i = 0; i < 8; i += 2) { dma1<WNT>(xb + i * 9216, wsrc, L0 + L_XT + sw * 8192 + i * 1024); dma1<WNT>(xb + (i + 1) * 9216, wsrc, L0 + L_XT + sw * 8192 + i * 1024 + 1024); waitvm<0>(); }
      lds_arrive(L, 1); lds_wait<8>(L, 1, 4);
#pragma unroll
      for (int s = 0; s < 8; s++) { LAS char* p = L + L_XT + s * 4096 + wrd + sw * 32; const u32x2 a = trread(p), c2 = trread(p + 512); wox[s] = u32x4{a[0], a[1], c2[0], c2[1]}; }
    }
    lds_arrive(L, 2);
    STAMP(5);
    lds_wait<8>(L, 2, 8);
    if (sw == 0) {
      const int hh = lane & 1, ww = (lane >> 1) & 3;
      float mw[4], lw[4];
#pragma unroll
      for (int i = 0; i < 4; i++) { const f32x2 v = *(LAS f32x2*)(L + L_ML + i * 16 + hh * 8); mw[i] = v[0]; lw[i] = v[1]; }
      const float Mc = fmaxf(fmaxf(mw[0], mw[1]), fmaxf(mw[2], mw[3])), Ms = (Mc == -INFINITY) ? 0.f : Mc;
      float Lc = 0;
#pragma unroll
      for (int i = 0; i < 4; i++) Lc += lw[i] * __builtin_amdgcn_exp2f((mw[i] - Ms) * 1.4426950408889634f);
      const unsigned tm = tag16(Mc, E), tl = tag10(Lc, E & 0x3ffu);
      const u32x4 rec = {dppu<0x00>(tm), dppu<0x55>(tm), dppu<0x00>(tl), dppu<0x55>(tl)};
      if (lane == 0) st128(rs, WS_ML + bid * 128, rec);
      u32x4 o;
      _Pragma("unroll 1") for (unsigned s = 0; s < SPIN_CAP; s++) {
        cfence();
        o = ld128(rs, WS_ML + (bid ^ 128) * 128);
        if (all_ok((((o[0] ^ E) | (o[1] ^ E)) & 0xffffu) | (((o[2] ^ E) | (o[3] ^ E)) & 0x3ffu))) break;
        __builtin_amdgcn_s_sleep(8);
      }
      cfence();
      const float Mo = hi2f(hh ? o[1] : o[0]), Lo = un10(hh ? o[3] : o[2]);
      const float M = fmaxf(Mc, Mo);
      const float Lt = un10(tl) * __builtin_amdgcn_exp2f((Mc - M) * 1.4426950408889634f) + Lo * __builtin_amdgcn_exp2f((Mo - M) * 1.4426950408889634f);
      const float mine = *(LAS float*)(L + L_ML + ww * 16 + hh * 8);
      if (lane < 8) *(LAS float*)(L + L_SC + lane * 4) = __builtin_amdgcn_exp2f((mine - M) * 1.4426950408889634f) / Lt;
      lds_arrive(L, 3);
    }
    if (idx < 24) {
      {
        const int l48 = lane < 48 ? lane : 47, bb = grp * 4 + sw;
        u32x4 sbv[3], cbv[3], gav, gbv;
        _Pragma("unroll 1") for (unsigned s = 0; s < SPIN_CAP; s++) {
          cfence(); unsigned bad = 0;
#pragma unroll
          for (int i = 0; i < 3; i++) { sbv[i] = ld128(rs, WS_MODR + (bb * 2304 + 576 + 192 * i + 4 * l48) * 4); cbv[i] = ld128(rs, WS_MODR + (bb * 2304 + 1152 + 192 * i + 4 * l48) * 4); }
          gav = ld128(rs, WS_MODR + (tb * 2304 + (task ? tcol : 0)) * 4); gbv = ld128(rs, WS_MODR + (tb * 2304 + 1728 + (task ? tcol : 0)) * 4);
#pragma unroll
          for (int i = 0; i < 3; i++) bad |= xor4(sbv[i], E) | xor4(cbv[i], E);
          if (all_ok((bad | xor4(gav, E) | xor4(gbv, E)) & 0xffffu)) break;
        }
        cfence();
#pragma unroll
        for (int i = 0; i < 6; i++) { g1p[i] = pk2(1.f + hi2f(cbv[i >> 1][(2 * i) & 3]), 1.f + hi2f(cbv[i >> 1][(2 * i + 1) & 3])); sbp[i] = hi2(sbv[i >> 1][(2 * i) & 3], sbv[i >> 1][(2 * i + 1) & 3]); pin(g1p[i]); pin(sbp[i]); }
        if (sw == 3 && task && lane < 8) { const u32x2 xr = *(const u32x2*)(x + (tb * 576 + tcol) * 2);
          *(LAS u32x4*)(L + L_TP + lane * 48) = gav & 0xffff0000u; *(LAS u32x4*)(L + L_TP + lane * 48 + 16) = gbv & 0xffff0000u;
          *(LAS u32x4*)(L + L_TP + lane * 48 + 32) = u32x4{xr[0] << 16, xr[0] & 0xffff0000u, xr[1] << 16, xr[1] & 0xffff0000u}; }
        if (sw == 2 && lane < 32) *(LAS unsigned*)(L + L_H1 + (lane >> 3) * H1_STR + 96 + (lane & 7) * 4) = 0;
      }
      unsigned o1[3];
#pragma unroll
      for (int i = 0; i < 3; i++) { const int u = 64 * i + lane; o1[i] = (u / 6) * 9216 + (u % 6) * 16; }
      const char* w1b = w1 + slice * 96; const char* w2b = w2 + (size_t)slice * 55296;
      _Pragma("unroll 1") for (int gi = sw; gi < 18; gi += 4) {
#pragma unroll
        for (int i = 0; i < 3; i++) { dma1<WNT>(w1b + (size_t)gi * 32 * 9216, o1[i], L0 + L_W1 + gi * 3072 + i * 1024); waitvm<W1W>(); }
      }
      _Pragma("unroll 1") for (int gi = 32 + sw; gi < 54; gi += 4) { dma1<WNT>(w2b + gi * 1024, lane * 16, L0 + L_W2 + gi * 1024); waitvm<2>(); }
      waitvm<0>();
      STAMP(14);
      lds_wait<2>(L, 7, 4);
      _Pragma("unroll 1") for (int gi = sw; gi < 32; gi += 4) { dma1<WNT>(w2b + gi * 1024, lane * 16, L0 + L_W2 + gi * 1024); waitvm<W2W>(); }
    }
    STAMP(6);
  }
  const int n4 = n >> 2;
  unsigned obase = (n & 3) * (4096 + 64) + OSLOT * 16384 + g * 16;
  int p1off = WS_P1 + ((b * 32 + kvh * 2 + half) * 576 + ((n & 1) ? 448 + 16 * w : 64 * w + 16 * n4) + 4 * g) * 4;
  if (!((n & 3) == 0 || (n == 1 && w >= 4))) p1off = WS_END;
  unsigned E10 = E & 0x3ffu;
  pin(obase); pin(p1off); pin(E10);
  wg_barrier();
  STAMP(7);
  if (w == 3 && lane < 2) st32(rs, WS_FP + (b * 2 + lane) * 128 + (kvh * 2 + half) * 4, E);
  {
    f32x4 d[4] = {{0, 0, 0, 0}, {0, 0, 0, 0}, {0, 0, 0, 0}, {0, 0, 0, 0}}, dx = {0, 0, 0, 0};
    LAS char* ob = L + obase;
    bf16x8 of[8];
#pragma unroll
    for (int s = 0; s < 8; s++) of[s] = *(LAS bf16x8*)(ob + s * 64);
#pragma unroll
    for (int s = 0; s < 8; s++)
#pragma unroll
      for (int u = 0; u < 4; u++) d[u] = mfma32(asbf8(wof[u][s]), of[s], d[u]);
    if (w >= 4) {
#pragma unroll
      for (int s = 0; s < 8; s++) dx = mfma32(asbf8(wox[s]), of[s], dx);
    }
    u32x4 y;
#pragma unroll
    for (int r = 0; r < 4; r++) {
      const float a = sel4(d[0][r], d[1][r], d[2][r], d[3][r]);
      const float ta = a + dppf<XOR1>(a), tb = dx[r] + dppf<XOR1>(dx[r]);
      float t = (n & 1) ? tb : ta; t += dppf<XOR2>(t);
      y[r] = tag10(t, E10);
    }
    st128(rs, p1off, y);
  }
  cfence();
  STAMP(8);
  if (idx < 24) {
    u32x4 w2a[5], w2b[5];
    auto w2read = [&]() {
      LAS char* pa = L + L_W2 + (8 * g + (n >> 2)) * 1152 + (n & 3) * 8;
      LAS char* pb = L + L_W2 + (32 + 8 * (g & 1) + (n >> 2)) * 1152 + (n & 3) * 8;
#pragma unroll
      for (int u = 0; u < 5; u++) { const int o = u < 4 ? w * 128 + u * 32 : 1024 + (w & 3) * 32;
        const u32x2 a = trread(pa + o), c2 = trread(pa + o + 4608); w2a[u] = u32x4{a[0], a[1], c2[0], c2[1]};
        const u32x2 a2 = trread(pb + o), c3 = trread(pb + o + 4608); w2b[u] = u32x4{a2[0], a2[1], c3[0], c3[1]}; } };
    f32x4 x1v, gbv;
    u32x4 w1f[18];
    if (w == 3) {
      if (task) {
        const f32x4 gav = *(LAS f32x4*)(L + L_TP + (lane & 7) * 48), xv = *(LAS f32x4*)(L + L_TP + (lane & 7) * 48 + 32);
        gbv = *(LAS f32x4*)(L + L_TP + (lane & 7) * 48 + 16);
        int po = WS_P1 + ((tb * 32 + p8) * 576 + tcol) * 4, xo = WS_X1 + p8 * X1_REP + (tb * 576 + tcol) * 4;
        pin(po); pin(xo);
        hint_wait(rs, WS_FP + (tb * 2 + (xcd & 1)) * 128 + (lane & 31) * 4, E);
        STAMP(5);
        if (DLY_B) __builtin_amdgcn_s_sleep(DLY_B);
        u32x4 p[4];
        _Pragma("unroll 1") for (unsigned s = 0; s < SPIN_CAP; s++) {
          cfence();
          unsigned bad = 0;
#pragma unroll
          for (int i = 0; i < 4; i++) p[i] = __builtin_amdgcn_raw_buffer_load_b128(rs, po, i * 8 * 2304, 16);
#pragma unroll
          for (int i = 0; i < 4; i++) bad |= xor4(p[i], E);
          if (all_ok(bad & 0x3ffu)) break;
        }
        cfence();
        STAMP(6);
        u32x4 y;
#pragma unroll
        for (int r = 0; r < 4; r++) {
          float s = (un10(p[0][r]) + un10(p[1][r])) + (un10(p[2][r]) + un10(p[3][r]));
          s += dppf<ROR8>(s); s = x16sum(s); s = x32sum(s);
          y[r] = tag16(xv[r] + rbf(gav[r] * rbf(s)), E); x1v[r] = hi2f(y[r]);
        }
        st128(rs, xo, y);
        cfence();
        STAMP(9);
      }
    } else if (w >= 4) {
      int xo = WS_X1 + xcd * X1_REP + ((grp * 4 + w - 4) * 576 + 4 * (lane < 48 ? lane : 47)) * 4;
      unsigned ha = L_H2 + (w - 4) * H2_STR + lane * 8;
      float g1[12], sb[12];
#pragma unroll
      for (int i = 0; i < 12; i++) { g1[i] = (i & 1) ? hi2f(g1p[i >> 1]) : bf2f(g1p[i >> 1]); sb[i] = (i & 1) ? hi2f(sbp[i >> 1]) : bf2f(sbp[i >> 1]); pin(g1[i]); pin(sb[i]); }
      pin(xo); pin(ha);
      u32x4 xv[3];
      _Pragma("unroll 1") for (unsigned s = 0; s < SPIN_CAP; s++) {
        cfence(); unsigned bad = 0;
#pragma unroll
        for (int i = 0; i < 3; i++) xv[i] = ld128(rs, xo + 768 * i);
#pragma unroll
        for (int i = 0; i < 3; i++) bad |= xor4(xv[i], E);
        if (all_ok(bad & 0xffffu)) break;
      }
      cfence();
      STAMP(10);
      const float x0 = hi2f(__builtin_amdgcn_readfirstlane(xv[0][0]));
      float xf[12], s1 = 0, s2 = 0;
#pragma unroll
      for (int i = 0; i < 12; i++) { xf[i] = hi2f(xv[i >> 2][i & 3]); const float dd = xf[i] - x0; s1 += dd; s2 += dd * dd; }
      if (lane >= 48) { s1 = 0; s2 = 0; }
      s1 += dppf<XOR1>(s1); s2 += dppf<XOR1>(s2); s1 += dppf<XOR2>(s1); s2 += dppf<XOR2>(s2);
      s1 += dppf<ROR4>(s1); s2 += dppf<ROR4>(s2); s1 += dppf<ROR8>(s1); s2 += dppf<ROR8>(s2);
      s1 = x16sum(s1); s2 = x16sum(s2); s1 = x32sum(s1); s2 = x32sum(s2);
      const float md = s1 * (1.f / 576.f), var = fmaxf(s2 * (1.f / 576.f) - md * md, 0.f);
      const float mb = rbf(x0 + md), rs_ = __builtin_amdgcn_rcpf(rbf(__builtin_amdgcn_sqrtf(rbf(rbf(var) + 1e-5f))));
      if (lane < 48) {
#pragma unroll
        for (int i = 0; i < 3; i++) { float hh[4];
#pragma unroll
          for (int j = 0; j < 4; j++) hh[j] = (xf[4 * i + j] - mb) * rs_ * g1[4 * i + j] + sb[4 * i + j];
          *(LAS u32x2*)(L + ha + 384 * i) = u32x2{pk2(hh[0], hh[1]), pk2(hh[2], hh[3])}; }
      }
    } else {
      LAS char* p = L + L_W1 + (8 * g + (n >> 2)) * 96 + (n & 3) * 8 + w * 32;
#pragma unroll
      for (int s = 0; s < 18; s++) { const u32x2 a = trread(p + s * 3072), c2 = trread(p + s * 3072 + 384); w1f[s] = u32x4{a[0], a[1], c2[0], c2[1]}; }
    }
    int po = WS_P2 + (((grp * 4 + (n & 3)) * 96 + slice) * 576 + 4 * g) * 4, po2 = n < 4 ? po + (448 + 16 * w) * 4 : WS_END;
    po += (64 * w + 16 * n4) * 4;
    int fo = WS_P2 + ((tb * 96 + 24 * w + p8) * 576 + tcol) * 4;
    unsigned h1a = L_H1 + (n & 3) * H1_STR + g * 16, h2a = L_H2 + (n & 3) * H2_STR + g * 16, h1w = L_H1 + n * H1_STR + (w * 16 + 4 * g) * 2;
    pin(po); pin(po2); pin(h1a); pin(h2a); pin(h1w); pin(fo);
    waitvm<0>();
    wg_barrier();
    STAMP(11);
    if (w >= 3) w2read();
    else {
      f32x4 d = {0, 0, 0, 0}; bf16x8 hf[18];
#pragma unroll
      for (int s = 0; s < 18; s++) hf[s] = *(LAS bf16x8*)(L + h2a + s * 64);
      cfence();
      w2read();
#pragma unroll
      for (int s = 0; s < 18; s++) d = mfma32(asbf8(w1f[s]), hf[s], d);
#pragma unroll
      for (int r = 0; r < 4; r++) d[r] = d[r] < 0.f ? 0.f : d[r];
      if (n < 4) *(LAS u32x2*)(L + h1w) = u32x2{pk2(d[0], d[1]), pk2(d[2], d[3])};
    }
    wg_barrier();
    STAMP(12);
    {
      const bf16x8 ha = *(LAS bf16x8*)(L + h1a), hb = *(LAS bf16x8*)(L + h1a + 64);
      f32x4 d[5];
#pragma unroll
      for (int u = 0; u < 4; u++) d[u] = mfma32(asbf8(w2b[u]), hb, mfma32(asbf8(w2a[u]), ha, f32x4{0, 0, 0, 0}));
      if (w >= 4) d[4] = mfma32(asbf8(w2b[4]), hb, mfma32(asbf8(w2a[4]), ha, f32x4{0, 0, 0, 0}));
      u32x4 y;
#pragma unroll
      for (int r = 0; r < 4; r++) y[r] = tag10(sel4(d[0][r], d[1][r], d[2][r], d[3][r]), E10);
      st128(rs, po, y);
      if (w >= 4) st128(rs, po2, u32x4{tag10(d[4][0], E10), tag10(d[4][1], E10), tag10(d[4][2], E10), tag10(d[4][3], E10)});
    }
    cfence();
    if (task && w < 4) {
      STAMP(13);
      __builtin_amdgcn_s_sleep(DLY_C);
      u32x4 p[3];
      _Pragma("unroll 1") for (unsigned s = 0; s < SPIN_CAP; s++) {
        cfence(); unsigned bad = 0;
#pragma unroll
        for (int i = 0; i < 3; i++) p[i] = __builtin_amdgcn_raw_buffer_load_b128(rs, fo, i * 8 * 2304, 16);
#pragma unroll
        for (int i = 0; i < 3; i++) bad |= xor4(p[i], E);
        if (all_ok(bad & 0x3ffu)) break;
      }
      cfence();
      STAMP(14);
      f32x4 s;
#pragma unroll
      for (int r = 0; r < 4; r++) s[r] = (un10(p[0][r]) + un10(p[1][r])) + un10(p[2][r]);
      if (w < 3) { *(LAS f32x4*)(L + L_W1 + w * 1024 + lane * 16) = s; lds_arrive(L, 6); }
      else {
        lds_wait<0>(L, 6, 3);
        s = (*(LAS f32x4*)(L + L_W1 + lane * 16) + *(LAS f32x4*)(L + L_W1 + 1024 + lane * 16)) + (*(LAS f32x4*)(L + L_W1 + 2048 + lane * 16) + s);
        float o[4];
#pragma unroll
        for (int r = 0; r < 4; r++) { float t = s[r]; t += dppf<ROR8>(t); t = x16sum(t); t = x32sum(t); o[r] = x1v[r] + rbf(gbv[r] * rbf(t)); }
        if (lane < 8) *(u32x2*)(out + (tb * 576 + tcol) * 2) = u32x2{pk2(o[0], o[1]), pk2(o[2], o[3])};
      }
    }
  } else if (bid == 248 && w == 1) {
    _Pragma("unroll 1") for (unsigned s = 0; s < SPIN_CAP; s++) {
      cfence();
      const u32x4 v = ld128(rs, WS_X1 + (lane & 7) * 576 * 4);
      if (all_ok(xor4(v, E) & 0xffffu)) break;
      __builtin_amdgcn_s_sleep(4);
    }
    cfence();
    if (lane == 0) st32(rs, WS_EPOCH, E);
  }
}

extern "C" void kernel_launch(void* const* d_in, const int* in_sizes, int n_in, void* d_out, int out_size, void* d_ws, size_t ws_size, hipStream_t stream) {
  fused_block<<<256, 512, 0, stream>>>((const char*)d_in[1], (const char*)d_in[2], (const char*)d_in[0], (const char*)d_in[3], (const char*)d_in[4], (const char*)d_in[6],
                                       (const char*)d_in[7], (const char*)d_in[8], (const char*)d_in[9], (char*)d_out, (char*)d_ws);
}
```

```cpp
#include <hip/hip_runtime.h>
#include <hip/hip_bf16.h>
#include <stdint.h>
typedef unsigned short u16;
typedef unsigned long long u64;
typedef short s16x4 __attribute__((ext_vector_type(4)));
typedef __bf16 bf16x8 __attribute__((ext_vector_type(8)));
typedef __bf16 bf16x2 __attribute__((ext_vector_type(2)));
typedef float f32x4 __attribute__((ext_vector_type(4)));
typedef float f32x2 __attribute__((ext_vector_type(2)));
typedef unsigned u32x4 __attribute__((ext_vector_type(4)));
typedef unsigned u32x2 __attribute__((ext_vector_type(2)));
#define LAS __attribute__((address_space(3)))
#define DEVI __device__ __forceinline__
#define STAMPS 0

constexpr int WS_EPOCH = 0;
constexpr int WS_FH = 1024, WS_FQ = 2048, WS_FP = 4096;
constexpr int WS_HG = 8192, HG_REP = 18 * 2048;
constexpr int WS_QG = WS_HG + 8 * HG_REP, QG_PL = 8 * 4096 * 4;
constexpr int WS_MODR = WS_QG + 6 * QG_PL;
constexpr int WS_ML = WS_MODR + 8 * 2304 * 4;
constexpr int WS_P1 = WS_ML + 256 * 128;
constexpr int WS_X1 = WS_P1 + 8 * 32 * 576 * 4, X1_REP = 8 * 576 * 4;
constexpr int WS_P2 = WS_X1 + 8 * X1_REP;
constexpr int WS_END = WS_P2 + 8 * 96 * 576 * 4;
constexpr int WS_STAMP = 4 << 20;
constexpr int L_W2 = 49152, L_W1 = L_W2 + 55296;
constexpr int L_STG = 131072, L_RED = 131072, L_NX = L_RED + 17408, L_XT = 98304;
constexpr int L_MISC = 159744;
constexpr int L_CNT = L_MISC, L_ML = L_MISC + 64, L_SC = L_MISC + 128, L_Q = L_MISC + 256, L_H1 = L_MISC + 1024, L_TP = L_MISC + 2048;
constexpr int H2_STR = 1216, H1_STR = 144;
constexpr int L_TOTAL = 163840;
constexpr unsigned SPIN_CAP = 400000;

DEVI float bf2f(unsigned v) { return __uint_as_float(v << 16); }
DEVI float hi2f(unsigned v) { return __uint_as_float(v & 0xffff0000u); }
DEVI unsigned pk2(float lo, float hi) { bf16x2 v = {(__bf16)lo, (__bf16)hi}; return __builtin_bit_cast(unsigned, v); }
DEVI float rbf(float f) { return hi2f(pk2(f, f)); }
template <int CTRL> DEVI unsigned dppu(unsigned x) { return (unsigned)__builtin_amdgcn_mov_dpp((int)x, CTRL, 0xf, 0xf, true); }
template <int CTRL> DEVI float dppf(float x) { return __uint_as_float(dppu<CTRL>(__float_as_uint(x))); }
constexpr int XOR1 = 0xB1, XOR2 = 0x4E, HMIR = 0x141, ROR4 = 0x124, ROR8 = 0x128;
DEVI float x16sum(float x) { auto s = __builtin_amdgcn_permlane16_swap(__float_as_uint(x), __float_as_uint(x), false, false); return __uint_as_float(s[0]) + __uint_as_float(s[1]); }
DEVI float x32sum(float x) { auto s = __builtin_amdgcn_permlane32_swap(__float_as_uint(x), __float_as_uint(x), false, false); return __uint_as_float(s[0]) + __uint_as_float(s[1]); }
DEVI float x16max(float x) { auto s = __builtin_amdgcn_permlane16_swap(__float_as_uint(x), __float_as_uint(x), false, false); return fmaxf(__uint_as_float(s[0]), __uint_as_float(s[1])); }
DEVI float x32max(float x) { auto s = __builtin_amdgcn_permlane32_swap(__float_as_uint(x), __float_as_uint(x), false, false); return fmaxf(__uint_as_float(s[0]), __uint_as_float(s[1])); }
DEVI unsigned lo2(unsigned a, unsigned b) { return __builtin_amdgcn_perm(b, a, 0x05040100u); }
DEVI unsigned hi2(unsigned a, unsigned b) { return __builtin_amdgcn_perm(b, a, 0x07060302u); }
DEVI bf16x8 asbf8(u32x4 v) { return __builtin_bit_cast(bf16x8, v); }
DEVI f32x4 mfma32(bf16x8 a, bf16x8 b, f32x4 c) { return __builtin_amdgcn_mfma_f32_16x16x32_bf16(a, b, c, 0, 0, 0); }
DEVI f32x4 mfma16(u32x2 a, u32x2 b, f32x4 c) { return __builtin_amdgcn_mfma_f32_16x16x16bf16_1k(__builtin_bit_cast(s16x4, a), __builtin_bit_cast(s16x4, b), c, 0, 0, 0); }
DEVI u32x2 trread(LAS char* p) { return __builtin_bit_cast(u32x2, __builtin_amdgcn_ds_read_tr16_b64_v4i16((LAS s16x4*)p)); }
DEVI void cfence() { asm volatile("" ::: "memory"); }
DEVI void wg_barrier() { asm volatile("s_waitcnt lgkmcnt(0)\n\ts_barrier" ::: "memory"); }
template <int N> DEVI void waitvm() { asm volatile("s_waitcnt vmcnt(%0)" ::"i"(N) : "memory"); }
DEVI void waitlgkm() { asm volatile("s_waitcnt lgkmcnt(0)" ::: "memory"); }
typedef __amdgpu_buffer_rsrc_t rsrc_t;
DEVI u32x4 ld128(rsrc_t rs, int off) { return __builtin_amdgcn_raw_buffer_load_b128(rs, off, 0, 16); }
DEVI void st128(rsrc_t rs, int off, u32x4 v) { __builtin_amdgcn_raw_buffer_store_b128(v, rs, off, 0, 16); }
DEVI rsrc_t mkrs(const char* p, int bytes) { return __builtin_amdgcn_make_buffer_rsrc((void*)p, 0, bytes, 0x00020000); }
template <int BANK> DEVI float banksel(float old, float v) { return __uint_as_float((unsigned)__builtin_amdgcn_update_dpp((int)__float_as_uint(old), (int)__float_as_uint(v), 0xE4, 0xf, 1 << BANK, false)); }
DEVI float sel4(float a, float b, float c, float d) { return banksel<3>(banksel<2>(banksel<1>(a, b), c), d); }
template <class T> DEVI void pin(T& v) { asm volatile("" : "+v"(v)); }
DEVI unsigned ld32(rsrc_t rs, int off) { return __builtin_amdgcn_raw_buffer_load_b32(rs, off, 0, 16); }
DEVI void st32(rsrc_t rs, int off, unsigned v) { __builtin_amdgcn_raw_buffer_store_b32(v, rs, off, 0, 16); }
DEVI unsigned xor4(u32x4 v, unsigned t) { return (v[0] ^ t) | (v[1] ^ t) | (v[2] ^ t) | (v[3] ^ t); }
DEVI bool all_ok(unsigned bad) { return __builtin_amdgcn_ballot_w64(bad != 0) == 0; }
DEVI unsigned tag16(float v, unsigned t) { return (pk2(v, v) & 0xffff0000u) | (t & 0xffffu); }
DEVI unsigned tag10(float v, unsigned t10) { return (__float_as_uint(v) & ~0x3ffu) | t10; }
DEVI float un10(unsigned v) { return __uint_as_float(v & ~0x3ffu); }
DEVI void hint_wait(rsrc_t rs, int off, unsigned E) {
  _Pragma("unroll 1") for (unsigned s = 0; s < SPIN_CAP; s++) { cfence(); if (all_ok(ld32(rs, off) ^ E)) break; }
  cfence();
}
DEVI void lds_arrive(LAS char* L, int i) { waitlgkm(); if ((threadIdx.x & 63) == 0) __hip_atomic_fetch_add((LAS unsigned*)(L + L_CNT + 4 * i), 1u, __ATOMIC_RELAXED, __HIP_MEMORY_SCOPE_WORKGROUP); cfence(); }
template <int SLEEP> DEVI void lds_wait(LAS char* L, int i, unsigned target) {
  _Pragma("unroll 1") for (unsigned s = 0; s < 16 * SPIN_CAP; s++) {
    unsigned v = *(volatile LAS unsigned*)(L + L_CNT + 4 * i);
    if (__builtin_amdgcn_readfirstlane(v) >= target) break;
    if (SLEEP) __builtin_amdgcn_s_sleep(SLEEP);
  }
  cfence();
}
#define DMA4_BODY(POL) asm volatile("s_mov_b32 %0, m0\n\ts_mov_b32 m0, %6\n\ts_nop 0\n\t" \
     "global_load_lds_dwordx4 %1, %5 offset:0" POL "\n\tglobal_load_lds_dwordx4 %2, %5 offset:1024" POL "\n\t" \
     "global_load_lds_dwordx4 %3, %5 offset:2048" POL "\n\tglobal_load_lds_dwordx4 %4, %5 offset:3072" POL "\n\ts_mov_b32 m0, %0" \
     : "=&s"(keep) : "v"(o[0]), "v"(o[1]), "v"(o[2]), "v"(o[3]), "s"(sbase), "s"(dst) : "memory")
template <int NT> DEVI void dma4(const char* sbase, const unsigned (&o)[4], unsigned dst) { unsigned keep; if (NT) DMA4_BODY(" nt"); else DMA4_BODY(""); }
template <int NT> DEVI void dma1(const char* sbase, unsigned o, unsigned dst) {
  unsigned keep;
  if (NT) asm volatile("s_mov_b32 %0, m0\n\ts_mov_b32 m0, %3\n\ts_nop 0\n\tglobal_load_lds_dwordx4 %1, %2 nt\n\ts_mov_b32 m0, %0" : "=&s"(keep) : "v"(o), "s"(sbase), "s"(dst) : "memory");
  else asm volatile("s_mov_b32 %0, m0\n\ts_mov_b32 m0, %3\n\ts_nop 0\n\tglobal_load_lds_dwordx4 %1, %2\n\ts_mov_b32 m0, %0" : "=&s"(keep) : "v"(o), "s"(sbase), "s"(dst) : "memory");
}
#define WNT 0
template <int R> DEVI unsigned ror(unsigned x) { if constexpr (R == 0) return x; else return dppu<0x120 + R>(x); }
template <int T8, int U> DEVI void pv1(f32x4& acc, unsigned z0, unsigned z1, u32x2 v) {
  constexpr int R = (2 * (U - T8) + 16) & 15;
  acc = mfma16(v, u32x2{ror<R>(z0), ror<R>(z1)}, acc);
}
#define NPRE 8
constexpr int NF = 8 + NPRE;
constexpr int slot_of(int t) { return t < NF ? t % 8 : (t - NF) % 3; }
#define W0 28
constexpr bool wiss(int t) { return t >= W0 && t < W0 + 32; }
template <int WT> DEVI void dma4_paced(const char* sbase, const unsigned (&o)[4], unsigned dst) { unsigned keep;
  asm volatile("s_mov_b32 %0, m0\n\ts_mov_b32 m0, %6\n\ts_waitcnt vmcnt(%7) lgkmcnt(0)\n\t"
     "global_load_lds_dwordx4 %1, %5 offset:0 nt\n\ts_waitcnt vmcnt(%7)\n\tglobal_load_lds_dwordx4 %2, %5 offset:1024 nt\n\ts_waitcnt vmcnt(%7)\n\t"
     "global_load_lds_dwordx4 %3, %5 offset:2048 nt\n\ts_waitcnt vmcnt(%7)\n\tglobal_load_lds_dwordx4 %4, %5 offset:3072 nt\n\ts_mov_b32 m0, %0"
     : "=&s"(keep) : "v"(o[0]), "v"(o[1]), "v"(o[2]), "v"(o[3]), "s"(sbase), "s"(dst), "i"(WT) : "memory"); }
#define W1W 1
#define W2W 14
#define DLY_B 2
#define DLY_C 12
#define WPACE 4
#define DLY_Q 8
#define PW 4
constexpr int OSLOT = slot_of(62), L_H2 = slot_of(63) * 16384;
static_assert(NPRE >= 4 && NPRE <= 8, "schedule");
template <int I, int N, class F> DEVI void sfor(F&& f) { if constexpr (I < N) { f(std::integral_constant<int, I>{}); sfor<I + 1, N>(f); } }
#define STAMP(i) do { if ((i) == 0) { pt0 = (unsigned)__builtin_amdgcn_s_memrealtime(); ptp = pt0 + (0); } else if ((i) == 99) ptp = (unsigned)__builtin_amdgcn_s_memrealtime(); } while (0)

__global__ void __launch_bounds__(512, 2) fused_block(const char* Kc, const char* Vc, const char* x, const char* wq, const char* wo, const char* w1,
                                                      const char* w2, const char* cc, const char* wm, char* out, char* ws) {
  __shared__ __attribute__((aligned(16))) char lds_mem[L_TOTAL];
  LAS char* L = (LAS char*)lds_mem;
  const unsigned L0 = (unsigned)(size_t)L;
  const int lane = threadIdx.x & 63, w = __builtin_amdgcn_readfirstlane(threadIdx.x >> 6);
  const int bid = blockIdx.x, xcd = bid & 7, idx = bid >> 3;
  const int kvh = 2 * xcd + (idx & 1), b = (idx >> 1) & 7, half = idx >> 4;
  const int n = lane & 15, g = lane >> 4;
  const rsrc_t rs = __builtin_amdgcn_make_buffer_rsrc((void*)ws, 0, WS_END, 0x00020000);
  unsigned pt0, ptp;
  STAMP(0);
  if (threadIdx.x < 12) *(LAS unsigned*)(L + L_CNT + threadIdx.x * 4) = 0;
  wg_barrier();
  u32x4 wof[4][8];
  u32x4 wox[8];
  unsigned E;
  unsigned g1p[6], sbp[6];
  const int i12 = idx % 12, grp = idx / 12, slice = 12 * xcd + i12;
  const bool task = idx < 24 && i12 < 9;
  const int tb = 4 * grp + (xcd >> 1), tcol = 32 * (9 * (xcd & 1) + i12) + 4 * (lane & 7), p8 = lane >> 3;
  const unsigned wsrc = (lane >> 3) * 1152 + (lane & 7) * 16;
  const char* wob = wo + (size_t)kvh * 256 * 1152 + w * 128;
  const int j1 = (idx - 24) * 8 + xcd;
  const bool s1 = idx >= 24 && j1 < 54;
  const unsigned wrd = (8 * g + (n >> 2)) * 128 + (n & 3) * 8;

  if (w < 4) {
    const size_t base = (size_t)(b * 16 + kvh) * 1048576 + (size_t)half * 524288 + w * 4096;
    const char* kb = Kc + base; const char* vb = Vc + base;
    unsigned ko[4], vo[4];
#pragma unroll
    for (int i = 0; i < 4; i++) { const int row = 4 * i + g; ko[i] = g * 256 + ((n ^ row) * 16); vo[i] = g * 256 + ((n ^ ((row & 7) << 1)) * 16); }
    const unsigned ring0 = L0 + w * 4096;
    LAS char* ring = L + w * 4096;
    unsigned kr[4];
#pragma unroll
    for (int s = 0; s < 4; s++) kr[s] = n * 256 + (((4 * s + g) ^ n) * 16);
    auto issue = [&](auto T) { constexpr int t = T; waitlgkm();
      if constexpr (t < 32) dma4<1>(kb + t * 16384, ko, ring0 + slot_of(t) * 16384);
      else if constexpr (t < 64) dma4<1>(vb + (t - 32) * 16384, vo, ring0 + slot_of(t) * 16384); };
    bf16x8 pre[NPRE][4];
    if (s1) lds_wait<0>(L, 4, 4);
    sfor<0, NF>([&](auto T) { constexpr int t = T;
      if (s1) dma4_paced<7>(kb + t * 16384, ko, ring0 + slot_of(t) * 16384); else dma4_paced<PW>(kb + t * 16384, ko, ring0 + slot_of(t) * 16384);
      if constexpr (t >= 2 && t - 2 < NPRE) {
#pragma unroll
        for (int s = 0; s < 4; s++) pre[t - 2][s] = *(LAS bf16x8*)(ring + (t - 2) * 16384 + kr[s]); } });
    const int vrow = 4 * g + (n >> 2), vp = n & 3, n2 = n >> 1;
    const unsigned vr0 = vrow * 256 + (vp >> 1) * 16 + (vp & 1) * 8, vx = (vrow & 7) << 5;
    STAMP(1);
    lds_wait<0>(L, 8, 1);
    STAMP(2);
    bf16x8 qf[4];
    f32x4 S[4];
    auto score = [&](auto T, const bf16x8 (&kf)[4]) { constexpr int t = T;
      f32x4 d = {0, 0, 0, 0};
#pragma unroll
      for (int s = 0; s < 4; s++) d = mfma32(kf[s], qf[s], d);
#pragma unroll
      for (int r = 0; r < 4; r++) S[t >> 3][r] = ((t & 7) == 0 || n2 == (t & 7)) ? d[r] : S[t >> 3][r]; };
    auto kread = [&](auto T, bf16x8 (&kf)[4]) { constexpr int t = T;
#pragma unroll
      for (int s = 0; s < 4; s++) kf[s] = *(LAS bf16x8*)(ring + slot_of(t) * 16384 + kr[s]); };
    auto wstep = [&](auto T) { constexpr int t = T;
      if constexpr (t == 63) waitvm<0>(); else if constexpr (wiss(t - 1)) waitvm<5>(); else waitvm<4>();
      issue(std::integral_constant<int, t + 2>{});
      if constexpr (wiss(t)) dma1<WNT>(wob + (t - W0) * 8 * 1152, wsrc, ring0 + (3 + (((t - W0) >> 2) & 1)) * 16384 + ((t - W0) & 3) * 1024); };
    auto wuse = [&](auto T) { constexpr int t = T;
      if constexpr (wiss(t - 2) && ((t - 2 - W0) & 3) == 3) { constexpr int s = (t - 2 - W0) >> 2;
#pragma unroll
        for (int u = 0; u < 4; u++) { LAS char* p = ring + (3 + (s & 1)) * 16384 + wrd + u * 32; const u32x2 a = trread(p), c2 = trread(p + 512); wof[u][s] = u32x4{a[0], a[1], c2[0], c2[1]}; }
        if constexpr (s == 7) lds_arrive(L, 7); } };
    {
      bf16x8 k8[4], k9[4];
      kread(std::integral_constant<int, 8>{}, k8); issue(std::integral_constant<int, NF>{});
      kread(std::integral_constant<int, 9>{}, k9); issue(std::integral_constant<int, NF + 1>{});
#pragma unroll
      for (int s = 0; s < 4; s++) qf[s] = *(LAS bf16x8*)(L + L_Q + (n & 1) * 256 + (4 * s + g) * 16);
      E = *(LAS unsigned*)(L + L_CNT + 60);
      score(std::integral_constant<int, 8>{}, k8); score(std::integral_constant<int, 9>{}, k9);
    }
    sfor<0, NPRE>([&](auto T) { score(T, pre[T]); });
    sfor<NPRE, 32>([&](auto T) { constexpr int t = T;
      if constexpr (t != 8 && t != 9) {
        if constexpr (t == NF - 2) waitvm<12>(); else if constexpr (t == NF - 1) waitvm<8>(); else if constexpr (t >= NF) wstep(T);
        bf16x8 kf[4]; kread(T, kf); score(T, kf); wuse(T);
        if constexpr (t == NF) lds_arrive(L, 5);
      } });
    STAMP(3);
    float m = -INFINITY;
#pragma unroll
    for (int G = 0; G < 4; G++)
#pragma unroll
      for (int r = 0; r < 4; r++) { const float s = rbf(rbf(S[G][r]) * 0.08838834764831845f); S[G][r] = s; m = fmaxf(m, s); }
    m = fmaxf(m, dppf<XOR2>(m)); m = fmaxf(m, dppf<ROR4>(m)); m = fmaxf(m, dppf<ROR8>(m)); m = x16max(m); m = x32max(m);
    const float msub = (m == -INFINITY) ? 0.f : m;
    float l = 0; unsigned P[4][2];
#pragma unroll
    for (int G = 0; G < 4; G++) { float e[4];
#pragma unroll
      for (int r = 0; r < 4; r++) { e[r] = __builtin_amdgcn_exp2f((S[G][r] - msub) * 1.4426950408889634f); l += e[r]; }
      P[G][0] = pk2(e[0], e[1]); P[G][1] = pk2(e[2], e[3]); }
    l += dppf<XOR2>(l); l += dppf<ROR4>(l); l += dppf<ROR8>(l); l = x16sum(l); l = x32sum(l);
    if (lane < 2) *(LAS f32x2*)(L + L_ML + w * 16 + lane * 8) = f32x2{m, l};
    lds_arrive(L, 2);
    f32x4 acc = {0, 0, 0, 0};
    float sc; unsigned oaddr;
    sfor<32, 64>([&](auto T) { constexpr int t = T; constexpr int T8 = t & 7, G = (t - 32) >> 3;
      wstep(T);
      u32x2 v[8];
#pragma unroll
      for (int u = 0; u < 8; u++) v[u] = trread(ring + slot_of(t) * 16384 + vr0 + ((u << 5) ^ vx));
      const unsigned z0 = (n2 == T8) ? P[G][0] : 0u, z1 = (n2 == T8) ? P[G][1] : 0u;
      sfor<0, 8>([&](auto U) { pv1<T8, U>(acc, z0, z1, v[U]); });
      wuse(T);
      if constexpr (t == 56) { lds_wait<1>(L, 3, 1); sc = *(LAS float*)(L + L_SC + w * 8 + (n & 1) * 4);
        oaddr = w * (4096 + 64) + OSLOT * 16384 + ((n & 1) * 128 + n2 * 16 + g * 4) * 2; pin(sc); pin(oaddr); }
    });
    STAMP(4);
    *(LAS u32x2*)(L + oaddr) = u32x2{pk2(acc[0] * sc, acc[1] * sc), pk2(acc[2] * sc, acc[3] * sc)};
  } else {
    const int sw = w - 4, ks = n >> 3, c8 = n & 7;
    const unsigned Ev = __builtin_amdgcn_raw_buffer_load_b32(rs, WS_EPOCH, 0, 16);
    u32x4 wr[3][8];
    f32x4 a[8];
#pragma unroll
    for (int e = 0; e < 8; e++) a[e] = f32x4{0, 0, 0, 0};
    auto xpose = [&](u32x4 (&r)[8]) { u32x4 f[8];
#pragma unroll
      for (int e = 0; e < 4; e++)
#pragma unroll
        for (int j = 0; j < 4; j++) { f[2 * e][j] = lo2(r[2 * j][e], r[2 * j + 1][e]); f[2 * e + 1][j] = hi2(r[2 * j][e], r[2 * j + 1][e]); }
#pragma unroll
      for (int e = 0; e < 8; e++) r[e] = f[e]; };
    auto mm8 = [&](u32x4 af, const u32x4 (&f)[8]) {
#pragma unroll
      for (int e = 0; e < 8; e++) a[e] = mfma32(asbf8(af), asbf8(f[e]), a[e]); };
    auto redwrite = [&]() {
      if ((g >> 1) == ks) { LAS char* p = L + L_RED + ((sw * 2 + ks) * 8 + (g & 1) * 4) * 272 + c8 * 32;
#pragma unroll
        for (int r = 0; r < 4; r++) { *(LAS f32x4*)(p + r * 272) = f32x4{a[0][r], a[1][r], a[2][r], a[3][r]}; *(LAS f32x4*)(p + r * 272 + 16) = f32x4{a[4][r], a[5][r], a[6][r], a[7][r]}; } }
      lds_arrive(L, 0); };
    const int krow = 64 * sw + 32 * ks + 8 * g;
    if (s1) {
      const rsrc_t rc = mkrs(cc, 9216), rx = mkrs(x, 9216), rm = mkrs(wm, 576 * 6912);
      const int co = (c8 * 576 + krow) * 2, wo_ = krow * 6912 + j1 * 128 + c8 * 16;
      const int fc = 32 * ks + 8 * g + 4 * (sw & 1);
      const bool isca = j1 >= 9 && j1 < 18;
      const int fb = 2 * sw + (g & 1), j24 = (g >> 1) * 16 + n;
      u32x4 cv[3], xv[3];
      cv[0] = __builtin_amdgcn_raw_buffer_load_b128(rc, co, 0, 0); cv[1] = __builtin_amdgcn_raw_buffer_load_b128(rc, co + 512, 0, 0);
      cv[2] = __builtin_amdgcn_raw_buffer_load_b128(rc, co + 1024, 0, 0);
#pragma unroll
      for (int i = 0; i < 3; i++) xv[i] = __builtin_amdgcn_raw_buffer_load_b128(rx, (isca && j24 < 24) ? fb * 1152 + (j24 + 24 * i) * 16 : 9216, 0, 0);
      const u32x2 xs = __builtin_amdgcn_raw_buffer_load_b64(rx, isca ? fb * 1152 + (j1 - 9) * 128 + n * 8 : 9216, 0, 0);
#pragma unroll
      for (int i = 0; i < 2; i++)
#pragma unroll
        for (int j = 0; j < 8; j++) wr[i][j] = __builtin_amdgcn_raw_buffer_load_b128(rm, wo_, (i * 256 + j) * 6912, 0);
      if (sw == 0) {
#pragma unroll
        for (int j = 0; j < 8; j++) wr[2][j] = __builtin_amdgcn_raw_buffer_load_b128(rm, wo_, (512 + j) * 6912, 0);
      }
      if (j1 >= 18) lds_arrive(L, 4);
      cfence();
      STAMP(13);
      E = __builtin_amdgcn_readfirstlane(Ev) + 1u;
      if (sw == 0 && j1 < 18 && lane < 8) st32(rs, WS_FH + lane * 128 + j1 * 4, E);
#pragma unroll
      for (int i = 0; i < 3; i++)
#pragma unroll
        for (int j = 0; j < 4; j++) { const float v0 = bf2f(cv[i][j]), v1 = hi2f(cv[i][j]);
          cv[i][j] = pk2(v0 * __builtin_amdgcn_rcpf(1.f + __expf(-v0)), v1 * __builtin_amdgcn_rcpf(1.f + __expf(-v1))); }
      if (isca) {
        float s = 0;
#pragma unroll
        for (int i = 0; i < 12; i++) s += bf2f(xv[i >> 2][i & 3]) + hi2f(xv[i >> 2][i & 3]);
        s += dppf<XOR1>(s); s += dppf<XOR2>(s); s += dppf<ROR4>(s); s += dppf<ROR8>(s); s = x32sum(s);
        const float mean = s * (1.f / 576.f); float v = 0;
#pragma unroll
        for (int i = 0; i < 12; i++) { const float d0 = bf2f(xv[i >> 2][i & 3]) - mean, d1 = hi2f(xv[i >> 2][i & 3]) - mean; v += d0 * d0; v += d1 * d1; }
        if (j24 >= 24) v = 0;
        v += dppf<XOR1>(v); v += dppf<XOR2>(v); v += dppf<ROR4>(v); v += dppf<ROR8>(v); v = x32sum(v);
        const float mb = rbf(mean), sd = rbf(sqrtf(rbf(rbf(v * (1.f / 576.f)) + 1e-5f)));
        f32x4 nx;
#pragma unroll
        for (int r = 0; r < 4; r++) nx[r] = rbf(rbf(((r & 1) ? hi2f(xs[r >> 1]) : bf2f(xs[r >> 1])) - mb) / sd);
        *(LAS f32x4*)(L + L_NX + fb * 272 + n * 16) = nx;
      }
      cfence();
      xpose(wr[0]); mm8(cv[0], wr[0]); xpose(wr[1]); mm8(cv[1], wr[1]);
      if (sw == 0) { xpose(wr[2]); mm8(cv[2], wr[2]); }
      STAMP(1);
      redwrite();
      lds_wait<0>(L, 0, 4);
      const f32x4 nx = *(LAS f32x4*)(L + L_NX + c8 * 272 + fc * 4);
      f32x4 s = {0, 0, 0, 0};
#pragma unroll
      for (int p = 0; p < 8; p++) s += *(LAS f32x4*)(L + L_RED + (p * 8 + c8) * 272 + fc * 4);
      u32x4 y;
#pragma unroll
      for (int r = 0; r < 4; r++) y[r] = tag16(isca ? nx[r] * rbf(1.f + rbf(s[r])) : s[r], E);
      if (j1 < 18) { const int o = WS_HG + (sw >> 1) * 4 * HG_REP + j1 * 2048 + (sw & 1) * 1024 + lane * 16;
#pragma unroll
        for (int i = 0; i < 4; i++) st128(rs, o + i * HG_REP, y);
        lds_arrive(L, 4); }
      else if (sw < 2) st128(rs, WS_MODR + (c8 * 2304 + (j1 - 18) * 64 + fc) * 4, y);
      STAMP(2);
    } else if (idx < 24 && sw < 3) {
      const rsrc_t rq = mkrs(wq, 576 * 8192);
      const int bl = 24 * xcd + idx, k3 = bl >> 6, qc = 64 * (bl & 63), k0 = 192 * k3 + krow;
#pragma unroll
      for (int j = 0; j < 8; j++) wr[0][j] = __builtin_amdgcn_raw_buffer_load_b128(rq, k0 * 8192 + qc * 2 + c8 * 16, j * 8192, 0);
      cfence();
      STAMP(13);
      E = __builtin_amdgcn_readfirstlane(Ev) + 1u;
      xpose(wr[0]);
#pragma unroll
      for (int e = 0; e < 8; e++) pin(wr[0][e]);
      STAMP(1);
      const int hoff = WS_HG + xcd * HG_REP + (3 * k3 + sw) * 2048 + lane * 16;
      hint_wait(rs, WS_FH + xcd * 128 + (3 * k3 + sw + 9 * (lane & 1)) * 4, E);
      u32x4 h[4];
      _Pragma("unroll 1") for (unsigned s = 0; s < SPIN_CAP; s++) {
        cfence();
        h[0] = ld128(rs, hoff); h[1] = ld128(rs, hoff + 1024); h[2] = ld128(rs, hoff + 9 * 2048); h[3] = ld128(rs, hoff + 9 * 2048 + 1024);
        if (all_ok((xor4(h[0], E) | xor4(h[1], E) | xor4(h[2], E) | xor4(h[3], E)) & 0xffffu)) break;
      }
      cfence();
      STAMP(3);
      u32x4 af;
#pragma unroll
      for (int j = 0; j < 4; j++) af[j] = pk2(hi2f(h[j >> 1][(2 * j) & 3]) + hi2f(h[2 + (j >> 1)][(2 * j) & 3]), hi2f(h[j >> 1][(2 * j + 1) & 3]) + hi2f(h[2 + (j >> 1)][(2 * j + 1) & 3]));
      mm8(af, wr[0]);
      redwrite();
      lds_wait<0>(L, 0, 3);
      if (sw == 2) { if (lane == 0) st32(rs, WS_FQ + ((bl >> 3) & 7) * 128 + (k3 * 8 + (bl & 7)) * 4, E); }
      else {
        f32x4 s = {0, 0, 0, 0};
#pragma unroll
        for (int p = 0; p < 6; p++) s += *(LAS f32x4*)(L + L_RED + (p * 8 + 4 * sw + g) * 272 + n * 16);
        u32x4 yh, yl;
#pragma unroll
        for (int r = 0; r < 4; r++) { const unsigned u = __float_as_uint(s[r]); yh[r] = (u & 0xffff0000u) | (E & 0xffffu); yl[r] = (u << 16) | (E & 0xffffu); }
        const int o = WS_QG + 2 * k3 * QG_PL + ((4 * sw + g) * 4096 + qc + 4 * n) * 4;
        st128(rs, o, yh); st128(rs, o + QG_PL, yl);
      }
      STAMP(2);
    } else {
      if (idx < 24) {
        const rsrc_t rm = mkrs(wm, 576 * 6912);
        const int l24 = lane >= 24 ? 1 : 0; unsigned pf[4];
#pragma unroll
        for (int i = 0; i < 4; i++) { const int j = xcd + 8 * (2 * i + l24);
          pf[i] = __builtin_amdgcn_raw_buffer_load_b32(rm, (lane < 48 && j < 54) ? (24 * idx + lane - 24 * l24) * 6912 + j * 128 : 576 * 6912, 0, 0); }
        asm volatile("" :: "v"(pf[0]), "v"(pf[1]), "v"(pf[2]), "v"(pf[3]));
      }
      E = __builtin_amdgcn_readfirstlane(Ev) + 1u;
    }
    lds_arrive(L, 9);
    if (sw == 3) {
      u32x4 qv[6];
      if (idx < 24) { lds_wait<1>(L, 0, 3); __builtin_amdgcn_s_sleep(DLY_Q); }
      else hint_wait(rs, WS_FQ + xcd * 128 + ((g < 3 ? g : 2) * 8 + (idx & 1) * 4 + (lane & 3)) * 4, E);
      _Pragma("unroll 1") for (unsigned s = 0; s < SPIN_CAP; s++) {
        cfence(); unsigned bad = 0;
#pragma unroll
        for (int i = 0; i < 6; i++) qv[i] = ld128(rs, WS_QG + i * QG_PL + (b * 4096 + kvh * 256 + 4 * lane) * 4);
#pragma unroll
        for (int i = 0; i < 6; i++) bad |= xor4(qv[i], E);
        if (all_ok(bad & 0xffffu)) break;
      }
      cfence();
      float qs[4];
#pragma unroll
      for (int r = 0; r < 4; r++) qs[r] = (__uint_as_float(hi2(qv[1][r], qv[0][r])) + __uint_as_float(hi2(qv[3][r], qv[2][r]))) + __uint_as_float(hi2(qv[5][r], qv[4][r]));
      *(LAS u32x2*)(L + L_Q + lane * 8) = u32x2{pk2(qs[0], qs[1]), pk2(qs[2], qs[3])};
      if (lane == 0) *(LAS unsigned*)(L + L_CNT + 60) = E;
      lds_arrive(L, 8);
    }
    STAMP(4);
    lds_wait<2>(L, 9, 4);
    {
      const unsigned stg0 = L0 + L_STG + sw * 4096; LAS char* stg = L + L_STG + sw * 4096;
      sfor<0, 8>([&](auto S_) { constexpr int s = S_;
        waitlgkm();
        dma1<WNT>(wob + (4 * s) * 9216, wsrc, stg0); dma1<WNT>(wob + (4 * s + 1) * 9216, wsrc, stg0 + 1024);
        if (WPACE == 2) waitvm<0>();
        dma1<WNT>(wob + (4 * s + 2) * 9216, wsrc, stg0 + 2048);
        if (WPACE == 3) waitvm<2>();
        dma1<WNT>(wob + (4 * s + 3) * 9216, wsrc, stg0 + 3072); waitvm<0>();
#pragma unroll
        for (int u = 0; u < 4; u++) { LAS char* p = stg + wrd + u * 32; const u32x2 a = trread(p), c2 = trread(p + 512); wof[u][s] = u32x4{a[0], a[1], c2[0], c2[1]}; }
      });
      lds_wait<8>(L, 5, 4);
      const char* xb = wo + (size_t)kvh * 256 * 1152 + 1024 + sw * 8 * 9216;
      _Pragma("unroll 1") for (int i = 0; i < 8; i += 2) { dma1<WNT>(xb + i * 9216, wsrc, L0 + L_XT + sw * 8192 + i * 1024); dma1<WNT>(xb + (i + 1) * 9216, wsrc, L0 + L_XT + sw * 8192 + i * 1024 + 1024); waitvm<0>(); }
      lds_arrive(L, 1); lds_wait<8>(L, 1, 4);
#pragma unroll
      for (int s = 0; s < 8; s++) { LAS char* p = L + L_XT + s * 4096 + wrd + sw * 32; const u32x2 a = trread(p), c2 = trread(p + 512); wox[s] = u32x4{a[0], a[1], c2[0], c2[1]}; }
    }
    lds_arrive(L, 2);
    STAMP(5);
    lds_wait<8>(L, 2, 8);
    if (sw == 0) {
      const int hh = lane & 1, ww = (lane >> 1) & 3;
      float mw[4], lw[4];
#pragma unroll
      for (int i = 0; i < 4; i++) { const f32x2 v = *(LAS f32x2*)(L + L_ML + i * 16 + hh * 8); mw[i] = v[0]; lw[i] = v[1]; }
      const float Mc = fmaxf(fmaxf(mw[0], mw[1]), fmaxf(mw[2], mw[3])), Ms = (Mc == -INFINITY) ? 0.f : Mc;
      float Lc = 0;
#pragma unroll
      for (int i = 0; i < 4; i++) Lc += lw[i] * __builtin_amdgcn_exp2f((mw[i] - Ms) * 1.4426950408889634f);
      const unsigned tm = tag16(Mc, E), tl = tag10(Lc, E & 0x3ffu);
      const u32x4 rec = {dppu<0x00>(tm), dppu<0x55>(tm), dppu<0x00>(tl), dppu<0x55>(tl)};
      if (lane == 0) st128(rs, WS_ML + bid * 128, rec);
      u32x4 o;
      _Pragma("unroll 1") for (unsigned s = 0; s < SPIN_CAP; s++) {
        cfence();
        o = ld128(rs, WS_ML + (bid ^ 128) * 128);
        if (all_ok((((o[0] ^ E) | (o[1] ^ E)) & 0xffffu) | (((o[2] ^ E) | (o[3] ^ E)) & 0x3ffu))) break;
        __builtin_amdgcn_s_sleep(8);
      }
      cfence();
      const float Mo = hi2f(hh ? o[1] : o[0]), Lo = un10(hh ? o[3] : o[2]);
      const float M = fmaxf(Mc, Mo);
      const float Lt = un10(tl) * __builtin_amdgcn_exp2f((Mc - M) * 1.4426950408889634f) + Lo * __builtin_amdgcn_exp2f((Mo - M) * 1.4426950408889634f);
      const float mine = *(LAS float*)(L + L_ML + ww * 16 + hh * 8);
      if (lane < 8) *(LAS float*)(L + L_SC + lane * 4) = __builtin_amdgcn_exp2f((mine - M) * 1.4426950408889634f) / Lt;
      lds_arrive(L, 3);
    }
    if (idx < 24) {
      {
        const int l48 = lane < 48 ? lane : 47, bb = grp * 4 + sw;
        u32x4 sbv[3], cbv[3], gav, gbv;
        _Pragma("unroll 1") for (unsigned s = 0; s < SPIN_CAP; s++) {
          cfence(); unsigned bad = 0;
#pragma unroll
          for (int i = 0; i < 3; i++) { sbv[i] = ld128(rs, WS_MODR + (bb * 2304 + 576 + 192 * i + 4 * l48) * 4); cbv[i] = ld128(rs, WS_MODR + (bb * 2304 + 1152 + 192 * i + 4 * l48) * 4); }
          gav = ld128(rs, WS_MODR + (tb * 2304 + (task ? tcol : 0)) * 4); gbv = ld128(rs, WS_MODR + (tb * 2304 + 1728 + (task ? tcol : 0)) * 4);
#pragma unroll
          for (int i = 0; i < 3; i++) bad |= xor4(sbv[i], E) | xor4(cbv[i], E);
          if (all_ok((bad | xor4(gav, E) | xor4(gbv, E)) & 0xffffu)) break;
        }
        cfence();
#pragma unroll
        for (int i = 0; i < 6; i++) { g1p[i] = pk2(1.f + hi2f(cbv[i >> 1][(2 * i) & 3]), 1.f + hi2f(cbv[i >> 1][(2 * i + 1) & 3])); sbp[i] = hi2(sbv[i >> 1][(2 * i) & 3], sbv[i >> 1][(2 * i + 1) & 3]); pin(g1p[i]); pin(sbp[i]); }
        if (sw == 3 && task && lane < 8) { const u32x2 xr = *(const u32x2*)(x + (tb * 576 + tcol) * 2);
          *(LAS u32x4*)(L + L_TP + lane * 48) = gav & 0xffff0000u; *(LAS u32x4*)(L + L_TP + lane * 48 + 16) = gbv & 0xffff0000u;
          *(LAS u32x4*)(L + L_TP + lane * 48 + 32) = u32x4{xr[0] << 16, xr[0] & 0xffff0000u, xr[1] << 16, xr[1] & 0xffff0000u}; }
        if (sw == 2 && lane < 32) *(LAS unsigned*)(L + L_H1 + (lane >> 3) * H1_STR + 96 + (lane & 7) * 4) = 0;
      }
      unsigned o1[3];
#pragma unroll
      for (int i = 0; i < 3; i++) { const int u = 64 * i + lane; o1[i] = (u / 6) * 9216 + (u % 6) * 16; }
      const char* w1b = w1 + slice * 96; const char* w2b = w2 + (size_t)slice * 55296;
      _Pragma("unroll 1") for (int gi = sw; gi < 18; gi += 4) {
#pragma unroll
        for (int i = 0; i < 3; i++) { dma1<WNT>(w1b + (size_t)gi * 32 * 9216, o1[i], L0 + L_W1 + gi * 3072 + i * 1024); waitvm<W1W>(); }
      }
      _Pragma("unroll 1") for (int gi = 32 + sw; gi < 54; gi += 4) { dma1<WNT>(w2b + gi * 1024, lane * 16, L0 + L_W2 + gi * 1024); waitvm<2>(); }
      waitvm<0>();
      STAMP(14);
      lds_wait<2>(L, 7, 4);
      _Pragma("unroll 1") for (int gi = sw; gi < 32; gi += 4) { dma1<WNT>(w2b + gi * 1024, lane * 16, L0 + L_W2 + gi * 1024); waitvm<W2W>(); }
    }
    STAMP(6);
  }
  const int n4 = n >> 2;
  unsigned obase = (n & 3) * (4096 + 64) + OSLOT * 16384 + g * 16;
  int p1off = WS_P1 + ((b * 32 + kvh * 2 + half) * 576 + ((n & 1) ? 448 + 16 * w : 64 * w + 16 * n4) + 4 * g) * 4;
  if (!((n & 3) == 0 || (n == 1 && w >= 4))) p1off = WS_END;
  unsigned E10 = E & 0x3ffu;
  pin(obase); pin(p1off); pin(E10);
  wg_barrier();
  STAMP(7);
  if (w == 3 && lane < 2) st32(rs, WS_FP + (b * 2 + lane) * 128 + (kvh * 2 + half) * 4, E);
  {
    f32x4 d[4] = {{0, 0, 0, 0}, {0, 0, 0, 0}, {0, 0, 0, 0}, {0, 0, 0, 0}}, dx = {0, 0, 0, 0};
    LAS char* ob = L + obase;
    bf16x8 of[8];
#pragma unroll
    for (int s = 0; s < 8; s++) of[s] = *(LAS bf16x8*)(ob + s * 64);
#pragma unroll
    for (int s = 0; s < 8; s++)
#pragma unroll
      for (int u = 0; u < 4; u++) d[u] = mfma32(asbf8(wof[u][s]), of[s], d[u]);
    if (w >= 4) {
#pragma unroll
      for (int s = 0; s < 8; s++) dx = mfma32(asbf8(wox[s]), of[s], dx);
    }
    u32x4 y;
#pragma unroll
    for (int r = 0; r < 4; r++) {
      const float a = sel4(d[0][r], d[1][r], d[2][r], d[3][r]);
      const float ta = a + dppf<XOR1>(a), tb = dx[r] + dppf<XOR1>(dx[r]);
      float t = (n & 1) ? tb : ta; t += dppf<XOR2>(t);
      y[r] = tag10(t, E10);
    }
    st128(rs, p1off, y);
  }
  cfence();
  STAMP(8);
  if (idx < 24) {
    u32x4 w2a[5], w2b[5];
    auto w2read = [&]() {
      LAS char* pa = L + L_W2 + (8 * g + (n >> 2)) * 1152 + (n & 3) * 8;
      LAS char* pb = L + L_W2 + (32 + 8 * (g & 1) + (n >> 2)) * 1152 + (n & 3) * 8;
#pragma unroll
      for (int u = 0; u < 5; u++) { const int o = u < 4 ? w * 128 + u * 32 : 1024 + (w & 3) * 32;
        const u32x2 a = trread(pa + o), c2 = trread(pa + o + 4608); w2a[u] = u32x4{a[0], a[1], c2[0], c2[1]};
        const u32x2 a2 = trread(pb + o), c3 = trread(pb + o + 4608); w2b[u] = u32x4{a2[0], a2[1], c3[0], c3[1]}; } };
    f32x4 x1v, gbv;
    u32x4 w1f[18];
    if (w == 3) {
      if (task) {
        const f32x4 gav = *(LAS f32x4*)(L + L_TP + (lane & 7) * 48), xv = *(LAS f32x4*)(L + L_TP + (lane & 7) * 48 + 32);
        gbv = *(LAS f32x4*)(L + L_TP + (lane & 7) * 48 + 16);
        int po = WS_P1 + ((tb * 32 + p8) * 576 + tcol) * 4, xo = WS_X1 + p8 * X1_REP + (tb * 576 + tcol) * 4;
        pin(po); pin(xo);
        hint_wait(rs, WS_FP + (tb * 2 + (xcd & 1)) * 128 + (lane & 31) * 4, E);
        STAMP(5);
        if (DLY_B) __builtin_amdgcn_s_sleep(DLY_B);
        u32x4 p[4];
        _Pragma("unroll 1") for (unsigned s = 0; s < SPIN_CAP; s++) {
          cfence();
          unsigned bad = 0;
#pragma unroll
          for (int i = 0; i < 4; i++) p[i] = __builtin_amdgcn_raw_buffer_load_b128(rs, po, i * 8 * 2304, 16);
#pragma unroll
          for (int i = 0; i < 4; i++) bad |= xor4(p[i], E);
          if (all_ok(bad & 0x3ffu)) break;
        }
        cfence();
        STAMP(6);
        u32x4 y;
#pragma unroll
        for (int r = 0; r < 4; r++) {
          float s = (un10(p[0][r]) + un10(p[1][r])) + (un10(p[2][r]) + un10(p[3][r]));
          s += dppf<ROR8>(s); s = x16sum(s); s = x32sum(s);
          y[r] = tag16(xv[r] + rbf(gav[r] * rbf(s)), E); x1v[r] = hi2f(y[r]);
        }
        st128(rs, xo, y);
        cfence();
        STAMP(9);
      }
    } else if (w >= 4) {
      int xo = WS_X1 + xcd * X1_REP + ((grp * 4 + w - 4) * 576 + 4 * (lane < 48 ? lane : 47)) * 4;
      unsigned ha = L_H2 + (w - 4) * H2_STR + lane * 8;
      float g1[12], sb[12];
#pragma unroll
      for (int i = 0; i < 12; i++) { g1[i] = (i & 1) ? hi2f(g1p[i >> 1]) : bf2f(g1p[i >> 1]); sb[i] = (i & 1) ? hi2f(sbp[i >> 1]) : bf2f(sbp[i >> 1]); pin(g1[i]); pin(sb[i]); }
      pin(xo); pin(ha);
      u32x4 xv[3];
      _Pragma("unroll 1") for (unsigned s = 0; s < SPIN_CAP; s++) {
        cfence(); unsigned bad = 0;
#pragma unroll
        for (int i = 0; i < 3; i++) xv[i] = ld128(rs, xo + 768 * i);
#pragma unroll
        for (int i = 0; i < 3; i++) bad |= xor4(xv[i], E);
        if (all_ok(bad & 0xffffu)) break;
      }
      cfence();
      STAMP(10);
      const float x0 = hi2f(__builtin_amdgcn_readfirstlane(xv[0][0]));
      float xf[12], s1 = 0, s2 = 0;
#pragma unroll
      for (int i = 0; i < 12; i++) { xf[i] = hi2f(xv[i >> 2][i & 3]); const float dd = xf[i] - x0; s1 += dd; s2 += dd * dd; }
      if (lane >= 48) { s1 = 0; s2 = 0; }
      s1 += dppf<XOR1>(s1); s2 += dppf<XOR1>(s2); s1 += dppf<XOR2>(s1); s2 += dppf<XOR2>(s2);
      s1 += dppf<ROR4>(s1); s2 += dppf<ROR4>(s2); s1 += dppf<ROR8>(s1); s2 += dppf<ROR8>(s2);
      s1 = x16sum(s1); s2 = x16sum(s2); s1 = x32sum(s1); s2 = x32sum(s2);
      const float md = s1 * (1.f / 576.f), var = fmaxf(s2 * (1.f / 576.f) - md * md, 0.f);
      const float mb = rbf(x0 + md), rs_ = __builtin_amdgcn_rcpf(rbf(__builtin_amdgcn_sqrtf(rbf(rbf(var) + 1e-5f))));
      if (lane < 48) {
#pragma unroll
        for (int i = 0; i < 3; i++) { float hh[4];
#pragma unroll
          for (int j = 0; j < 4; j++) hh[j] = (xf[4 * i + j] - mb) * rs_ * g1[4 * i + j] + sb[4 * i + j];
          *(LAS u32x2*)(L + ha + 384 * i) = u32x2{pk2(hh[0], hh[1]), pk2(hh[2], hh[3])}; }
      }
    } else {
      LAS char* p = L + L_W1 + (8 * g + (n >> 2)) * 96 + (n & 3) * 8 + w * 32;
#pragma unroll
      for (int s = 0; s < 18; s++) { const u32x2 a = trread(p + s * 3072), c2 = trread(p + s * 3072 + 384); w1f[s] = u32x4{a[0], a[1], c2[0], c2[1]}; }
    }
    int po = WS_P2 + (((grp * 4 + (n & 3)) * 96 + slice) * 576 + 4 * g) * 4, po2 = n < 4 ? po + (448 + 16 * w) * 4 : WS_END;
    po += (64 * w + 16 * n4) * 4;
    int fo = WS_P2 + ((tb * 96 + 24 * w + p8) * 576 + tcol) * 4;
    unsigned h1a = L_H1 + (n & 3) * H1_STR + g * 16, h2a = L_H2 + (n & 3) * H2_STR + g * 16, h1w = L_H1 + n * H1_STR + (w * 16 + 4 * g) * 2;
    pin(po); pin(po2); pin(h1a); pin(h2a); pin(h1w); pin(fo);
    waitvm<0>();
    wg_barrier();
    STAMP(11);
    if (w >= 3) w2read();
    else {
      f32x4 d = {0, 0, 0, 0}; bf16x8 hf[18];
#pragma unroll
      for (int s = 0; s < 18; s++) hf[s] = *(LAS bf16x8*)(L + h2a + s * 64);
      cfence();
      w2read();
#pragma unroll
      for (int s = 0; s < 18; s++) d = mfma32(asbf8(w1f[s]), hf[s], d);
#pragma unroll
      for (int r = 0; r < 4; r++) d[r] = d[r] < 0.f ? 0.f : d[r];
      if (n < 4) *(LAS u32x2*)(L + h1w) = u32x2{pk2(d[0], d[1]), pk2(d[2], d[3])};
    }
    wg_barrier();
    STAMP(12);
    {
      const bf16x8 ha = *(LAS bf16x8*)(L + h1a), hb = *(LAS bf16x8*)(L + h1a + 64);
      f32x4 d[5];
#pragma unroll
      for (int u = 0; u < 4; u++) d[u] = mfma32(asbf8(w2b[u]), hb, mfma32(asbf8(w2a[u]), ha, f32x4{0, 0, 0, 0}));
      if (w >= 4) d[4] = mfma32(asbf8(w2b[4]), hb, mfma32(asbf8(w2a[4]), ha, f32x4{0, 0, 0, 0}));
      u32x4 y;
#pragma unroll
      for (int r = 0; r < 4; r++) y[r] = tag10(sel4(d[0][r], d[1][r], d[2][r], d[3][r]), E10);
      st128(rs, po, y);
      if (w >= 4) st128(rs, po2, u32x4{tag10(d[4][0], E10), tag10(d[4][1], E10), tag10(d[4][2], E10), tag10(d[4][3], E10)});
    }
    cfence();
    if (task && w < 4) {
      STAMP(13);
      __builtin_amdgcn_s_sleep(DLY_C);
      u32x4 p[3];
      _Pragma("unroll 1") for (unsigned s = 0; s < SPIN_CAP; s++) {
        cfence(); unsigned bad = 0;
#pragma unroll
        for (int i = 0; i < 3; i++) p[i] = __builtin_amdgcn_raw_buffer_load_b128(rs, fo, i * 8 * 2304, 16);
#pragma unroll
        for (int i = 0; i < 3; i++) bad |= xor4(p[i], E);
        if (all_ok(bad & 0x3ffu)) break;
      }
      cfence();
      STAMP(14);
      f32x4 s;
#pragma unroll
      for (int r = 0; r < 4; r++) s[r] = (un10(p[0][r]) + un10(p[1][r])) + un10(p[2][r]);
      if (w < 3) { *(LAS f32x4*)(L + L_W1 + w * 1024 + lane * 16) = s; lds_arrive(L, 6); }
      else {
        lds_wait<0>(L, 6, 3);
        s = (*(LAS f32x4*)(L + L_W1 + lane * 16) + *(LAS f32x4*)(L + L_W1 + 1024 + lane * 16)) + (*(LAS f32x4*)(L + L_W1 + 2048 + lane * 16) + s);
        float o[4];
#pragma unroll
        for (int r = 0; r < 4; r++) { float t = s[r]; t += dppf<ROR8>(t); t = x16sum(t); t = x32sum(t); o[r] = x1v[r] + rbf(gbv[r] * rbf(t)); }
        if (lane < 8) *(u32x2*)(out + (tb * 576 + tcol) * 2) = u32x2{pk2(o[0], o[1]), pk2(o[2], o[3])};
      }
    }
  } else if (bid == 248 && w == 1) {
    _Pragma("unroll 1") for (unsigned s = 0; s < SPIN_CAP; s++) {
      cfence();
      const u32x4 v = ld128(rs, WS_X1 + (lane & 7) * 576 * 4);
      if (all_ok(xor4(v, E) & 0xffffu)) break;
      __builtin_amdgcn_s_sleep(4);
    }
    cfence();
    if (lane == 0) st32(rs, WS_EPOCH, E);
  }
  STAMP(15);
  if (w == 0) { const unsigned tend = pt0 + 9000 + (1) * (int)(ptp - pt0); _Pragma("unroll 1") while ((int)((unsigned)__builtin_amdgcn_s_memrealtime() - tend) < 0) __builtin_amdgcn_s_sleep(1); }
}

extern "C" void kernel_launch(void* const* d_in, const int* in_sizes, int n_in, void* d_out, int out_size, void* d_ws, size_t ws_size, hipStream_t stream) {
  fused_block<<<256, 512, 0, stream>>>((const char*)d_in[1], (const char*)d_in[2], (const char*)d_in[0], (const char*)d_in[3], (const char*)d_in[4], (const char*)d_in[6],
                                       (const char*)d_in[7], (const char*)d_in[8], (const char*)d_in[9], (char*)d_out, (char*)d_ws);
}
```

```cpp
#include <hip/hip_runtime.h>
#include <hip/hip_bf16.h>
#include <stdint.h>
typedef unsigned short u16;
typedef unsigned long long u64;
typedef short s16x4 __attribute__((ext_vector_type(4)));
typedef __bf16 bf16x8 __attribute__((ext_vector_type(8)));
typedef __bf16 bf16x2 __attribute__((ext_vector_type(2)));
typedef float f32x4 __attribute__((ext_vector_type(4)));
typedef float f32x2 __attribute__((ext_vector_type(2)));
typedef unsigned u32x4 __attribute__((ext_vector_type(4)));
typedef unsigned u32x2 __attribute__((ext_vector_type(2)));
#define LAS __attribute__((address_space(3)))
#define DEVI __device__ __forceinline__
#define STAMPS 0

constexpr int WS_EPOCH = 0;
constexpr int WS_FH = 1024, WS_FQ = 2048, WS_FP = 4096;
constexpr int WS_HG = 8192, HG_REP = 18 * 2048;
constexpr int WS_QG = WS_HG + 8 * HG_REP, QG_PL = 8 * 4096 * 4;
constexpr int WS_MODR = WS_QG + 6 * QG_PL;
constexpr int WS_ML = WS_MODR + 8 * 2304 * 4;
constexpr int WS_P1 = WS_ML + 256 * 128;
constexpr int WS_X1 = WS_P1 + 8 * 32 * 576 * 4, X1_REP = 8 * 576 * 4;
constexpr int WS_P2 = WS_X1 + 8 * X1_REP;
constexpr int WS_END = WS_P2 + 8 * 96 * 576 * 4;
constexpr int WS_STAMP = 4 << 20;
constexpr int L_W2 = 49152, L_W1 = L_W2 + 55296;
constexpr int L_STG = 131072, L_RED = 131072, L_NX = L_RED + 17408, L_XT = 98304;
constexpr int L_MISC = 159744;
constexpr int L_CNT = L_MISC, L_ML = L_MISC + 64, L_SC = L_MISC + 128, L_Q = L_MISC + 256, L_H1 = L_MISC + 1024, L_TP = L_MISC + 2048;
constexpr int H2_STR = 1216, H1_STR = 144;
constexpr int L_TOTAL = 163840;
constexpr unsigned SPIN_CAP = 400000;

DEVI float bf2f(unsigned v) { return __uint_as_float(v << 16); }
DEVI float hi2f(unsigned v) { return __uint_as_float(v & 0xffff0000u); }
DEVI unsigned pk2(float lo, float hi) { bf16x2 v = {(__bf16)lo, (__bf16)hi}; return __builtin_bit_cast(unsigned, v); }
DEVI float rbf(float f) { return hi2f(pk2(f, f)); }
template <int CTRL> DEVI unsigned dppu(unsigned x) { return (unsigned)__builtin_amdgcn_mov_dpp((int)x, CTRL, 0xf, 0xf, true); }
template <int CTRL> DEVI float dppf(float x) { return __uint_as_float(dppu<CTRL>(__float_as_uint(x))); }
constexpr int XOR1 = 0xB1, XOR2 = 0x4E, HMIR = 0x141, ROR4 = 0x124, ROR8 = 0x128;
DEVI float x16sum(float x) { auto s = __builtin_amdgcn_permlane16_swap(__float_as_uint(x), __float_as_uint(x), false, false); return __uint_as_float(s[0]) + __uint_as_float(s[1]); }
DEVI float x32sum(float x) { auto s = __builtin_amdgcn_permlane32_swap(__float_as_uint(x), __float_as_uint(x), false, false); return __uint_as_float(s[0]) + __uint_as_float(s[1]); }
DEVI float x16max(float x) { auto s = __builtin_amdgcn_permlane16_swap(__float_as_uint(x), __float_as_uint(x), false, false); return fmaxf(__uint_as_float(s[0]), __uint_as_float(s[1])); }
DEVI float x32max(float x) { auto s = __builtin_amdgcn_permlane32_swap(__float_as_uint(x), __float_as_uint(x), false, false); return fmaxf(__uint_as_float(s[0]), __uint_as_float(s[1])); }
DEVI unsigned lo2(unsigned a, unsigned b) { return __builtin_amdgcn_perm(b, a, 0x05040100u); }
DEVI unsigned hi2(unsigned a, unsigned b) { return __builtin_amdgcn_perm(b, a, 0x07060302u); }
DEVI bf16x8 asbf8(u32x4 v) { return __builtin_bit_cast(bf16x8, v); }
DEVI f32x4 mfma32(bf16x8 a, bf16x8 b, f32x4 c) { return __builtin_amdgcn_mfma_f32_16x16x32_bf16(a, b, c, 0, 0, 0); }
DEVI f32x4 mfma16(u32x2 a, u32x2 b, f32x4 c) { return __builtin_amdgcn_mfma_f32_16x16x16bf16_1k(__builtin_bit_cast(s16x4, a), __builtin_bit_cast(s16x4, b), c, 0, 0, 0); }
DEVI u32x2 trread(LAS char* p) { return __builtin_bit_cast(u32x2, __builtin_amdgcn_ds_read_tr16_b64_v4i16((LAS s16x4*)p)); }
DEVI void cfence() { asm volatile("" ::: "memory"); }
DEVI void wg_barrier() { asm volatile("s_waitcnt lgkmcnt(0)\n\ts_barrier" ::: "memory"); }
template <int N> DEVI void waitvm() { asm volatile("s_waitcnt vmcnt(%0)" ::"i"(N) : "memory"); }
DEVI void waitlgkm() { asm volatile("s_waitcnt lgkmcnt(0)" ::: "memory"); }
typedef __amdgpu_buffer_rsrc_t rsrc_t;
DEVI u32x4 ld128(rsrc_t rs, int off) { return __builtin_amdgcn_raw_buffer_load_b128(rs, off, 0, 16); }
DEVI void st128(rsrc_t rs, int off, u32x4 v) { __builtin_amdgcn_raw_buffer_store_b128(v, rs, off, 0, 16); }
DEVI rsrc_t mkrs(const char* p, int bytes) { return __builtin_amdgcn_make_buffer_rsrc((void*)p, 0, bytes, 0x00020000); }
template <int BANK> DEVI float banksel(float old, float v) { return __uint_as_float((unsigned)__builtin_amdgcn_update_dpp((int)__float_as_uint(old), (int)__float_as_uint(v), 0xE4, 0xf, 1 << BANK, false)); }
DEVI float sel4(float a, float b, float c, float d) { return banksel<3>(banksel<2>(banksel<1>(a, b), c), d); }
template <class T> DEVI void pin(T& v) { asm volatile("" : "+v"(v)); }
DEVI unsigned ld32(rsrc_t rs, int off) { return __builtin_amdgcn_raw_buffer_load_b32(rs, off, 0, 16); }
DEVI void st32(rsrc_t rs, int off, unsigned v) { __builtin_amdgcn_raw_buffer_store_b32(v, rs, off, 0, 16); }
DEVI unsigned xor4(u32x4 v, unsigned t) { return (v[0] ^ t) | (v[1] ^ t) | (v[2] ^ t) | (v[3] ^ t); }
DEVI bool all_ok(unsigned bad) { return __builtin_amdgcn_ballot_w64(bad != 0) == 0; }
DEVI unsigned tag16(float v, unsigned t) { return (pk2(v, v) & 0xffff0000u) | (t & 0xffffu); }
DEVI unsigned tag10(float v, unsigned t10) { return (__float_as_uint(v) & ~0x3ffu) | t10; }
DEVI float un10(unsigned v) { return __uint_as_float(v & ~0x3ffu); }
DEVI void hint_wait(rsrc_t rs, int off, unsigned E) {
  _Pragma("unroll 1") for (unsigned s = 0; s < SPIN_CAP; s++) { cfence(); if (all_ok(ld32(rs, off) ^ E)) break; }
  cfence();
}
DEVI void lds_arrive(LAS char* L, int i) { waitlgkm(); if ((threadIdx.x & 63) == 0) __hip_atomic_fetch_add((LAS unsigned*)(L + L_CNT + 4 * i), 1u, __ATOMIC_RELAXED, __HIP_MEMORY_SCOPE_WORKGROUP); cfence(); }
template <int SLEEP> DEVI void lds_wait(LAS char* L, int i, unsigned target) {
  _Pragma("unroll 1") for (unsigned s = 0; s < 16 * SPIN_CAP; s++) {
    unsigned v = *(volatile LAS unsigned*)(L + L_CNT + 4 * i);
    if (__builtin_amdgcn_readfirstlane(v) >= target) break;
    if (SLEEP) __builtin_amdgcn_s_sleep(SLEEP);
  }
  cfence();
}
#define DMA4_BODY(POL) asm volatile("s_mov_b32 %0, m0\n\ts_mov_b32 m0, %6\n\ts_nop 0\n\t" \
     "global_load_lds_dwordx4 %1, %5 offset:0" POL "\n\tglobal_load_lds_dwordx4 %2, %5 offset:1024" POL "\n\t" \
     "global_load_lds_dwordx4 %3, %5 offset:2048" POL "\n\tglobal_load_lds_dwordx4 %4, %5 offset:3072" POL "\n\ts_mov_b32 m0, %0" \
     : "=&s"(keep) : "v"(o[0]), "v"(o[1]), "v"(o[2]), "v"(o[3]), "s"(sbase), "s"(dst) : "memory")
template <int NT> DEVI void dma4(const char* sbase, const unsigned (&o)[4], unsigned dst) { unsigned keep; if (NT) DMA4_BODY(" nt"); else DMA4_BODY(""); }
template <int NT> DEVI void dma1(const char* sbase, unsigned o, unsigned dst) {
  unsigned keep;
  if (NT) asm volatile("s_mov_b32 %0, m0\n\ts_mov_b32 m0, %3\n\ts_nop 0\n\tglobal_load_lds_dwordx4 %1, %2 nt\n\ts_mov_b32 m0, %0" : "=&s"(keep) : "v"(o), "s"(sbase), "s"(dst) : "memory");
  else asm volatile("s_mov_b32 %0, m0\n\ts_mov_b32 m0, %3\n\ts_nop 0\n\tglobal_load_lds_dwordx4 %1, %2\n\ts_mov_b32 m0, %0" : "=&s"(keep) : "v"(o), "s"(sbase), "s"(dst) : "memory");
}
#define WNT 0
template <int R> DEVI unsigned ror(unsigned x) { if constexpr (R == 0) return x; else return dppu<0x120 + R>(x); }
template <int T8, int U> DEVI void pv1(f32x4& acc, unsigned z0, unsigned z1, u32x2 v) {
  constexpr int R = (2 * (U - T8) + 16) & 15;
  acc = mfma16(v, u32x2{ror<R>(z0), ror<R>(z1)}, acc);
}
#define NPRE 8
constexpr int NF = 8 + NPRE;
constexpr int slot_of(int t) { return t < NF ? t % 8 : (t - NF) % 3; }
#define W0 28
constexpr bool wiss(int t) { return t >= W0 && t < W0 + 32; }
template <int WT> DEVI void dma4_paced(const char* sbase, const unsigned (&o)[4], unsigned dst) { unsigned keep;
  asm volatile("s_mov_b32 %0, m0\n\ts_mov_b32 m0, %6\n\ts_waitcnt vmcnt(%7) lgkmcnt(0)\n\t"
     "global_load_lds_dwordx4 %1, %5 offset:0 nt\n\ts_waitcnt vmcnt(%7)\n\tglobal_load_lds_dwordx4 %2, %5 offset:1024 nt\n\ts_waitcnt vmcnt(%7)\n\t"
     "global_load_lds_dwordx4 %3, %5 offset:2048 nt\n\ts_waitcnt vmcnt(%7)\n\tglobal_load_lds_dwordx4 %4, %5 offset:3072 nt\n\ts_mov_b32 m0, %0"
     : "=&s"(keep) : "v"(o[0]), "v"(o[1]), "v"(o[2]), "v"(o[3]), "s"(sbase), "s"(dst), "i"(WT) : "memory"); }
#define W1W 1
#define W2W 14
#define DLY_B 2
#define DLY_C 12
#define WPACE 4
#define DLY_Q 8
#define PW 4
constexpr int OSLOT = slot_of(62), L_H2 = slot_of(63) * 16384;
static_assert(NPRE >= 4 && NPRE <= 8, "schedule");
template <int I, int N, class F> DEVI void sfor(F&& f) { if constexpr (I < N) { f(std::integral_constant<int, I>{}); sfor<I + 1, N>(f); } }
#define STAMP(i) do { if ((i) == 0) { pt0 = (unsigned)__builtin_amdgcn_s_memrealtime(); ptp = pt0 + (0); } else if ((i) == 99) ptp = (unsigned)__builtin_amdgcn_s_memrealtime(); } while (0)

__global__ void __launch_bounds__(512, 2) fused_block(const char* Kc, const char* Vc, const char* x, const char* wq, const char* wo, const char* w1,
                                                      const char* w2, const char* cc, const char* wm, char* out, char* ws) {
  __shared__ __attribute__((aligned(16))) char lds_mem[L_TOTAL];
  LAS char* L = (LAS char*)lds_mem;
  const unsigned L0 = (unsigned)(size_t)L;
  const int lane = threadIdx.x & 63, w = __builtin_amdgcn_readfirstlane(threadIdx.x >> 6);
  const int bid = blockIdx.x, xcd = bid & 7, idx = bid >> 3;
  const int kvh = 2 * xcd + (idx & 1), b = (idx >> 1) & 7, half = idx >> 4;
  const int n = lane & 15, g = lane >> 4;
  const rsrc_t rs = __builtin_amdgcn_make_buffer_rsrc((void*)ws, 0, WS_END, 0x00020000);
  unsigned pt0, ptp;
  STAMP(0);
  if (threadIdx.x < 12) *(LAS unsigned*)(L + L_CNT + threadIdx.x * 4) = 0;
  wg_barrier();
  u32x4 wof[4][8];
  u32x4 wox[8];
  unsigned E;
  unsigned g1p[6], sbp[6];
  const int i12 = idx % 12, grp = idx / 12, slice = 12 * xcd + i12;
  const bool task = idx < 24 && i12 < 9;
  const int tb = 4 * grp + (xcd >> 1), tcol = 32 * (9 * (xcd & 1) + i12) + 4 * (lane & 7), p8 = lane >> 3;
  const unsigned wsrc = (lane >> 3) * 1152 + (lane & 7) * 16;
  const char* wob = wo + (size_t)kvh * 256 * 1152 + w * 128;
  const int j1 = (idx - 24) * 8 + xcd;
  const bool s1 = idx >= 24 && j1 < 54;
  const unsigned wrd = (8 * g + (n >> 2)) * 128 + (n & 3) * 8;

  if (w < 4) {
    const size_t base = (size_t)(b * 16 + kvh) * 1048576 + (size_t)half * 524288 + w * 4096;
    const char* kb = Kc + base; const char* vb = Vc + base;
    unsigned ko[4], vo[4];
#pragma unroll
    for (int i = 0; i < 4; i++) { const int row = 4 * i + g; ko[i] = g * 256 + ((n ^ row) * 16); vo[i] = g * 256 + ((n ^ ((row & 7) << 1)) * 16); }
    const unsigned ring0 = L0 + w * 4096;
    LAS char* ring = L + w * 4096;
    unsigned kr[4];
#pragma unroll
    for (int s = 0; s < 4; s++) kr[s] = n * 256 + (((4 * s + g) ^ n) * 16);
    auto issue = [&](auto T) { constexpr int t = T; waitlgkm();
      if constexpr (t < 32) dma4<1>(kb + t * 16384, ko, ring0 + slot_of(t) * 16384);
      else if constexpr (t < 64) dma4<1>(vb + (t - 32) * 16384, vo, ring0 + slot_of(t) * 16384); };
    bf16x8 pre[NPRE][4];
    if (s1) lds_wait<0>(L, 4, 4);
    sfor<0, NF>([&](auto T) { constexpr int t = T;
      if (s1) dma4_paced<7>(kb + t * 16384, ko, ring0 + slot_of(t) * 16384); else dma4_paced<PW>(kb + t * 16384, ko, ring0 + slot_of(t) * 16384);
      if constexpr (t >= 2 && t - 2 < NPRE) {
#pragma unroll
        for (int s = 0; s < 4; s++) pre[t - 2][s] = *(LAS bf16x8*)(ring + (t - 2) * 16384 + kr[s]); } });
    const int vrow = 4 * g + (n >> 2), vp = n & 3, n2 = n >> 1;
    const unsigned vr0 = vrow * 256 + (vp >> 1) * 16 + (vp & 1) * 8, vx = (vrow & 7) << 5;
    STAMP(1);
    lds_wait<0>(L, 8, 1);
    STAMP(2);
    bf16x8 qf[4];
    f32x4 S[4];
    auto score = [&](auto T, const bf16x8 (&kf)[4]) { constexpr int t = T;
      f32x4 d = {0, 0, 0, 0};
#pragma unroll
      for (int s = 0; s < 4; s++) d = mfma32(kf[s], qf[s], d);
#pragma unroll
      for (int r = 0; r < 4; r++) S[t >> 3][r] = ((t & 7) == 0 || n2 == (t & 7)) ? d[r] : S[t >> 3][r]; };
    auto kread = [&](auto T, bf16x8 (&kf)[4]) { constexpr int t = T;
#pragma unroll
      for (int s = 0; s < 4; s++) kf[s] = *(LAS bf16x8*)(ring + slot_of(t) * 16384 + kr[s]); };
    auto wstep = [&](auto T) { constexpr int t = T;
      if constexpr (t == 63) waitvm<0>(); else if constexpr (wiss(t - 1)) waitvm<5>(); else waitvm<4>();
      issue(std::integral_constant<int, t + 2>{});
      if constexpr (wiss(t)) dma1<WNT>(wob + (t - W0) * 8 * 1152, wsrc, ring0 + (3 + (((t - W0) >> 2) & 1)) * 16384 + ((t - W0) & 3) * 1024); };
    auto wuse = [&](auto T) { constexpr int t = T;
      if constexpr (wiss(t - 2) && ((t - 2 - W0) & 3) == 3) { constexpr int s = (t - 2 - W0) >> 2;
#pragma unroll
        for (int u = 0; u < 4; u++) { LAS char* p = ring + (3 + (s & 1)) * 16384 + wrd + u * 32; const u32x2 a = trread(p), c2 = trread(p + 512); wof[u][s] = u32x4{a[0], a[1], c2[0], c2[1]}; }
        if constexpr (s == 7) lds_arrive(L, 7); } };
    {
      bf16x8 k8[4], k9[4];
      kread(std::integral_constant<int, 8>{}, k8); issue(std::integral_constant<int, NF>{});
      kread(std::integral_constant<int, 9>{}, k9); issue(std::integral_constant<int, NF + 1>{});
#pragma unroll
      for (int s = 0; s < 4; s++) qf[s] = *(LAS bf16x8*)(L + L_Q + (n & 1) * 256 + (4 * s + g) * 16);
      E = *(LAS unsigned*)(L + L_CNT + 60);
      score(std::integral_constant<int, 8>{}, k8); score(std::integral_constant<int, 9>{}, k9);
    }
    sfor<0, NPRE>([&](auto T) { score(T, pre[T]); });
    sfor<NPRE, 32>([&](auto T) { constexpr int t = T;
      if constexpr (t != 8 && t != 9) {
        if constexpr (t == NF - 2) waitvm<12>(); else if constexpr (t == NF - 1) waitvm<8>(); else if constexpr (t >= NF) wstep(T);
        bf16x8 kf[4]; kread(T, kf); score(T, kf); wuse(T);
        if constexpr (t == NF) lds_arrive(L, 5);
      } });
    STAMP(3);
    float m = -INFINITY;
#pragma unroll
    for (int G = 0; G < 4; G++)
#pragma unroll
      for (int r = 0; r < 4; r++) { const float s = rbf(rbf(S[G][r]) * 0.08838834764831845f); S[G][r] = s; m = fmaxf(m, s); }
    m = fmaxf(m, dppf<XOR2>(m)); m = fmaxf(m, dppf<ROR4>(m)); m = fmaxf(m, dppf<ROR8>(m)); m = x16max(m); m = x32max(m);
    const float msub = (m == -INFINITY) ? 0.f : m;
    float l = 0; unsigned P[4][2];
#pragma unroll
    for (int G = 0; G < 4; G++) { float e[4];
#pragma unroll
      for (int r = 0; r < 4; r++) { e[r] = __builtin_amdgcn_exp2f((S[G][r] - msub) * 1.4426950408889634f); l += e[r]; }
      P[G][0] = pk2(e[0], e[1]); P[G][1] = pk2(e[2], e[3]); }
    l += dppf<XOR2>(l); l += dppf<ROR4>(l); l += dppf<ROR8>(l); l = x16sum(l); l = x32sum(l);
    if (lane < 2) *(LAS f32x2*)(L + L_ML + w * 16 + lane * 8) = f32x2{m, l};
    lds_arrive(L, 2);
    f32x4 acc = {0, 0, 0, 0};
    float sc; unsigned oaddr;
    sfor<32, 64>([&](auto T) { constexpr int t = T; constexpr int T8 = t & 7, G = (t - 32) >> 3;
      wstep(T);
      u32x2 v[8];
#pragma unroll
      for (int u = 0; u < 8; u++) v[u] = trread(ring + slot_of(t) * 16384 + vr0 + ((u << 5) ^ vx));
      const unsigned z0 = (n2 == T8) ? P[G][0] : 0u, z1 = (n2 == T8) ? P[G][1] : 0u;
      sfor<0, 8>([&](auto U) { pv1<T8, U>(acc, z0, z1, v[U]); });
      wuse(T);
      if constexpr (t == 56) { lds_wait<1>(L, 3, 1); sc = *(LAS float*)(L + L_SC + w * 8 + (n & 1) * 4);
        oaddr = w * (4096 + 64) + OSLOT * 16384 + ((n & 1) * 128 + n2 * 16 + g * 4) * 2; pin(sc); pin(oaddr); }
    });
    STAMP(4);
    *(LAS u32x2*)(L + oaddr) = u32x2{pk2(acc[0] * sc, acc[1] * sc), pk2(acc[2] * sc, acc[3] * sc)};
  } else {
    const int sw = w - 4, ks = n >> 3, c8 = n & 7;
    const unsigned Ev = __builtin_amdgcn_raw_buffer_load_b32(rs, WS_EPOCH, 0, 16);
    u32x4 wr[3][8];
    f32x4 a[8];
#pragma unroll
    for (int e = 0; e < 8; e++) a[e] = f32x4{0, 0, 0, 0};
    auto xpose = [&](u32x4 (&r)[8]) { u32x4 f[8];
#pragma unroll
      for (int e = 0; e < 4; e++)
#pragma unroll
        for (int j = 0; j < 4; j++) { f[2 * e][j] = lo2(r[2 * j][e], r[2 * j + 1][e]); f[2 * e + 1][j] = hi2(r[2 * j][e], r[2 * j + 1][e]); }
#pragma unroll
      for (int e = 0; e < 8; e++) r[e] = f[e]; };
    auto mm8 = [&](u32x4 af, const u32x4 (&f)[8]) {
#pragma unroll
      for (int e = 0; e < 8; e++) a[e] = mfma32(asbf8(af), asbf8(f[e]), a[e]); };
    auto redwrite = [&]() {
      if ((g >> 1) == ks) { LAS char* p = L + L_RED + ((sw * 2 + ks) * 8 + (g & 1) * 4) * 272 + c8 * 32;
#pragma unroll
        for (int r = 0; r < 4; r++) { *(LAS f32x4*)(p + r * 272) = f32x4{a[0][r], a[1][r], a[2][r], a[3][r]}; *(LAS f32x4*)(p + r * 272 + 16) = f32x4{a[4][r], a[5][r], a[6][r], a[7][r]}; } }
      lds_arrive(L, 0); };
    const int krow = 64 * sw + 32 * ks + 8 * g;
    if (s1) {
      const rsrc_t rc = mkrs(cc, 9216), rx = mkrs(x, 9216), rm = mkrs(wm, 576 * 6912);
      const int co = (c8 * 576 + krow) * 2, wo_ = krow * 6912 + j1 * 128 + c8 * 16;
      const int fc = 32 * ks + 8 * g + 4 * (sw & 1);
      const bool isca = j1 >= 9 && j1 < 18;
      const int fb = 2 * sw + (g & 1), j24 = (g >> 1) * 16 + n;
      u32x4 cv[3], xv[3];
      cv[0] = __builtin_amdgcn_raw_buffer_load_b128(rc, co, 0, 0); cv[1] = __builtin_amdgcn_raw_buffer_load_b128(rc, co + 512, 0, 0);
      cv[2] = __builtin_amdgcn_raw_buffer_load_b128(rc, co + 1024, 0, 0);
#pragma unroll
      for (int i = 0; i < 3; i++) xv[i] = __builtin_amdgcn_raw_buffer_load_b128(rx, (isca && j24 < 24) ? fb * 1152 + (j24 + 24 * i) * 16 : 9216, 0, 0);
      const u32x2 xs = __builtin_amdgcn_raw_buffer_load_b64(rx, isca ? fb * 1152 + (j1 - 9) * 128 + n * 8 : 9216, 0, 0);
#pragma unroll
      for (int i = 0; i < 2; i++)
#pragma unroll
        for (int j = 0; j < 8; j++) wr[i][j] = __builtin_amdgcn_raw_buffer_load_b128(rm, wo_, (i * 256 + j) * 6912, 0);
      if (sw == 0) {
#pragma unroll
        for (int j = 0; j < 8; j++) wr[2][j] = __builtin_amdgcn_raw_buffer_load_b128(rm, wo_, (512 + j) * 6912, 0);
      }
      if (j1 >= 18) lds_arrive(L, 4);
      cfence();
      STAMP(13);
      E = __builtin_amdgcn_readfirstlane(Ev) + 1u;
      if (sw == 0 && j1 < 18 && lane < 8) st32(rs, WS_FH + lane * 128 + j1 * 4, E);
#pragma unroll
      for (int i = 0; i < 3; i++)
#pragma unroll
        for (int j = 0; j < 4; j++) { const float v0 = bf2f(cv[i][j]), v1 = hi2f(cv[i][j]);
          cv[i][j] = pk2(v0 * __builtin_amdgcn_rcpf(1.f + __expf(-v0)), v1 * __builtin_amdgcn_rcpf(1.f + __expf(-v1))); }
      if (isca) {
        float s = 0;
#pragma unroll
        for (int i = 0; i < 12; i++) s += bf2f(xv[i >> 2][i & 3]) + hi2f(xv[i >> 2][i & 3]);
        s += dppf<XOR1>(s); s += dppf<XOR2>(s); s += dppf<ROR4>(s); s += dppf<ROR8>(s); s = x32sum(s);
        const float mean = s * (1.f / 576.f); float v = 0;
#pragma unroll
        for (int i = 0; i < 12; i++) { const float d0 = bf2f(xv[i >> 2][i & 3]) - mean, d1 = hi2f(xv[i >> 2][i & 3]) - mean; v += d0 * d0; v += d1 * d1; }
        if (j24 >= 24) v = 0;
        v += dppf<XOR1>(v); v += dppf<XOR2>(v); v += dppf<ROR4>(v); v += dppf<ROR8>(v); v = x32sum(v);
        const float mb = rbf(mean), sd = rbf(sqrtf(rbf(rbf(v * (1.f / 576.f)) + 1e-5f)));
        f32x4 nx;
#pragma unroll
        for (int r = 0; r < 4; r++) nx[r] = rbf(rbf(((r & 1) ? hi2f(xs[r >> 1]) : bf2f(xs[r >> 1])) - mb) / sd);
        *(LAS f32x4*)(L + L_NX + fb * 272 + n * 16) = nx;
      }
      cfence();
      xpose(wr[0]); mm8(cv[0], wr[0]); xpose(wr[1]); mm8(cv[1], wr[1]);
      if (sw == 0) { xpose(wr[2]); mm8(cv[2], wr[2]); }
      STAMP(1);
      redwrite();
      lds_wait<0>(L, 0, 4);
      const f32x4 nx = *(LAS f32x4*)(L + L_NX + c8 * 272 + fc * 4);
      f32x4 s = {0, 0, 0, 0};
#pragma unroll
      for (int p = 0; p < 8; p++) s += *(LAS f32x4*)(L + L_RED + (p * 8 + c8) * 272 + fc * 4);
      u32x4 y;
#pragma unroll
      for (int r = 0; r < 4; r++) y[r] = tag16(isca ? nx[r] * rbf(1.f + rbf(s[r])) : s[r], E);
      if (j1 < 18) { const int o = WS_HG + (sw >> 1) * 4 * HG_REP + j1 * 2048 + (sw & 1) * 1024 + lane * 16;
#pragma unroll
        for (int i = 0; i < 4; i++) st128(rs, o + i * HG_REP, y);
        lds_arrive(L, 4); }
      else if (sw < 2) st128(rs, WS_MODR + (c8 * 2304 + (j1 - 18) * 64 + fc) * 4, y);
      STAMP(2);
    } else if (idx < 24 && sw < 3) {
      const rsrc_t rq = mkrs(wq, 576 * 8192);
      const int bl = 24 * xcd + idx, k3 = bl >> 6, qc = 64 * (bl & 63), k0 = 192 * k3 + krow;
#pragma unroll
      for (int j = 0; j < 8; j++) wr[0][j] = __builtin_amdgcn_raw_buffer_load_b128(rq, k0 * 8192 + qc * 2 + c8 * 16, j * 8192, 0);
      cfence();
      STAMP(13);
      E = __builtin_amdgcn_readfirstlane(Ev) + 1u;
      xpose(wr[0]);
#pragma unroll
      for (int e = 0; e < 8; e++) pin(wr[0][e]);
      STAMP(1);
      const int hoff = WS_HG + xcd * HG_REP + (3 * k3 + sw) * 2048 + lane * 16;
      hint_wait(rs, WS_FH + xcd * 128 + (3 * k3 + sw + 9 * (lane & 1)) * 4, E);
      u32x4 h[4];
      _Pragma("unroll 1") for (unsigned s = 0; s < SPIN_CAP; s++) {
        cfence();
        h[0] = ld128(rs, hoff); h[1] = ld128(rs, hoff + 1024); h[2] = ld128(rs, hoff + 9 * 2048); h[3] = ld128(rs, hoff + 9 * 2048 + 1024);
        if (all_ok((xor4(h[0], E) | xor4(h[1], E) | xor4(h[2], E) | xor4(h[3], E)) & 0xffffu)) break;
      }
      cfence();
      STAMP(3);
      u32x4 af;
#pragma unroll
      for (int j = 0; j < 4; j++) af[j] = pk2(hi2f(h[j >> 1][(2 * j) & 3]) + hi2f(h[2 + (j >> 1)][(2 * j) & 3]), hi2f(h[j >> 1][(2 * j + 1) & 3]) + hi2f(h[2 + (j >> 1)][(2 * j + 1) & 3]));
      mm8(af, wr[0]);
      redwrite();
      lds_wait<0>(L, 0, 3);
      if (sw == 2) { if (lane == 0) st32(rs, WS_FQ + ((bl >> 3) & 7) * 128 + (k3 * 8 + (bl & 7)) * 4, E); }
      else {
        f32x4 s = {0, 0, 0, 0};
#pragma unroll
        for (int p = 0; p < 6; p++) s += *(LAS f32x4*)(L + L_RED + (p * 8 + 4 * sw + g) * 272 + n * 16);
        u32x4 yh, yl;
#pragma unroll
        for (int r = 0; r < 4; r++) { const unsigned u = __float_as_uint(s[r]); yh[r] = (u & 0xffff0000u) | (E & 0xffffu); yl[r] = (u << 16) | (E & 0xffffu); }
        const int o = WS_QG + 2 * k3 * QG_PL + ((4 * sw + g) * 4096 + qc + 4 * n) * 4;
        st128(rs, o, yh); st128(rs, o + QG_PL, yl);
      }
      STAMP(2);
    } else {
      if (idx < 24) {
        const rsrc_t rm = mkrs(wm, 576 * 6912);
        const int l24 = lane >= 24 ? 1 : 0; unsigned pf[4];
#pragma unroll
        for (int i = 0; i < 4; i++) { const int j = xcd + 8 * (2 * i + l24);
          pf[i] = __builtin_amdgcn_raw_buffer_load_b32(rm, (lane < 48 && j < 54) ? (24 * idx + lane - 24 * l24) * 6912 + j * 128 : 576 * 6912, 0, 0); }
        asm volatile("" :: "v"(pf[0]), "v"(pf[1]), "v"(pf[2]), "v"(pf[3]));
      }
      E = __builtin_amdgcn_readfirstlane(Ev) + 1u;
    }
    lds_arrive(L, 9);
    if (sw == 3) {
      u32x4 qv[6];
      if (idx < 24) { lds_wait<1>(L, 0, 3); __builtin_amdgcn_s_sleep(DLY_Q); }
      else hint_wait(rs, WS_FQ + xcd * 128 + ((g < 3 ? g : 2) * 8 + (idx & 1) * 4 + (lane & 3)) * 4, E);
      _Pragma("unroll 1") for (unsigned s = 0; s < SPIN_CAP; s++) {
        cfence(); unsigned bad = 0;
#pragma unroll
        for (int i = 0; i < 6; i++) qv[i] = ld128(rs, WS_QG + i * QG_PL + (b * 4096 + kvh * 256 + 4 * lane) * 4);
#pragma unroll
        for (int i = 0; i < 6; i++) bad |= xor4(qv[i], E);
        if (all_ok(bad & 0xffffu)) break;
      }
      cfence();
      float qs[4];
#pragma unroll
      for (int r = 0; r < 4; r++) qs[r] = (__uint_as_float(hi2(qv[1][r], qv[0][r])) + __uint_as_float(hi2(qv[3][r], qv[2][r]))) + __uint_as_float(hi2(qv[5][r], qv[4][r]));
      *(LAS u32x2*)(L + L_Q + lane * 8) = u32x2{pk2(qs[0], qs[1]), pk2(qs[2], qs[3])};
      if (lane == 0) *(LAS unsigned*)(L + L_CNT + 60) = E;
      lds_arrive(L, 8);
    }
    STAMP(4);
    lds_wait<2>(L, 9, 4);
    {
      const unsigned stg0 = L0 + L_STG + sw * 4096; LAS char* stg = L + L_STG + sw * 4096;
      sfor<0, 8>([&](auto S_) { constexpr int s = S_;
        waitlgkm();
        dma1<WNT>(wob + (4 * s) * 9216, wsrc, stg0); dma1<WNT>(wob + (4 * s + 1) * 9216, wsrc, stg0 + 1024);
        if (WPACE == 2) waitvm<0>();
        dma1<WNT>(wob + (4 * s + 2) * 9216, wsrc, stg0 + 2048);
        if (WPACE == 3) waitvm<2>();
        dma1<WNT>(wob + (4 * s + 3) * 9216, wsrc, stg0 + 3072); waitvm<0>();
#pragma unroll
        for (int u = 0; u < 4; u++) { LAS char* p = stg + wrd + u * 32; const u32x2 a = trread(p), c2 = trread(p + 512); wof[u][s] = u32x4{a[0], a[1], c2[0], c2[1]}; }
      });
      lds_wait<8>(L, 5, 4);
      const char* xb = wo + (size_t)kvh * 256 * 1152 + 1024 + sw * 8 * 9216;
      _Pragma("unroll 1") for (int i = 0; i < 8; i += 2) { dma1<WNT>(xb + i * 9216, wsrc, L0 + L_XT + sw * 8192 + i * 1024); dma1<WNT>(xb + (i + 1) * 9216, wsrc, L0 + L_XT + sw * 8192 + i * 1024 + 1024); waitvm<0>(); }
      lds_arrive(L, 1); lds_wait<8>(L, 1, 4);
#pragma unroll
      for (int s = 0; s < 8; s++) { LAS char* p = L + L_XT + s * 4096 + wrd + sw * 32; const u32x2 a = trread(p), c2 = trread(p + 512); wox[s] = u32x4{a[0], a[1], c2[0], c2[1]}; }
    }
    lds_arrive(L, 2);
    STAMP(5);
    lds_wait<8>(L, 2, 8);
    if (sw == 0) {
      const int hh = lane & 1, ww = (lane >> 1) & 3;
      float mw[4], lw[4];
#pragma unroll
      for (int i = 0; i < 4; i++) { const f32x2 v = *(LAS f32x2*)(L + L_ML + i * 16 + hh * 8); mw[i] = v[0]; lw[i] = v[1]; }
      const float Mc = fmaxf(fmaxf(mw[0], mw[1]), fmaxf(mw[2], mw[3])), Ms = (Mc == -INFINITY) ? 0.f : Mc;
      float Lc = 0;
#pragma unroll
      for (int i = 0; i < 4; i++) Lc += lw[i] * __builtin_amdgcn_exp2f((mw[i] - Ms) * 1.4426950408889634f);
      const unsigned tm = tag16(Mc, E), tl = tag10(Lc, E & 0x3ffu);
      const u32x4 rec = {dppu<0x00>(tm), dppu<0x55>(tm), dppu<0x00>(tl), dppu<0x55>(tl)};
      if (lane == 0) st128(rs, WS_ML + bid * 128, rec);
      u32x4 o;
      _Pragma("unroll 1") for (unsigned s = 0; s < SPIN_CAP; s++) {
        cfence();
        o = ld128(rs, WS_ML + (bid ^ 128) * 128);
        if (all_ok((((o[0] ^ E) | (o[1] ^ E)) & 0xffffu) | (((o[2] ^ E) | (o[3] ^ E)) & 0x3ffu))) break;
        __builtin_amdgcn_s_sleep(8);
      }
      cfence();
      const float Mo = hi2f(hh ? o[1] : o[0]), Lo = un10(hh ? o[3] : o[2]);
      const float M = fmaxf(Mc, Mo);
      const float Lt = un10(tl) * __builtin_amdgcn_exp2f((Mc - M) * 1.4426950408889634f) + Lo * __builtin_amdgcn_exp2f((Mo - M) * 1.4426950408889634f);
      const float mine = *(LAS float*)(L + L_ML + ww * 16 + hh * 8);
      if (lane < 8) *(LAS float*)(L + L_SC + lane * 4) = __builtin_amdgcn_exp2f((mine - M) * 1.4426950408889634f) / Lt;
      lds_arrive(L, 3);
    }
    if (idx < 24) {
      {
        const int l48 = lane < 48 ? lane : 47, bb = grp * 4 + sw;
        u32x4 sbv[3], cbv[3], gav, gbv;
        _Pragma("unroll 1") for (unsigned s = 0; s < SPIN_CAP; s++) {
          cfence(); unsigned bad = 0;
#pragma unroll
          for (int i = 0; i < 3; i++) { sbv[i] = ld128(rs, WS_MODR + (bb * 2304 + 576 + 192 * i + 4 * l48) * 4); cbv[i] = ld128(rs, WS_MODR + (bb * 2304 + 1152 + 192 * i + 4 * l48) * 4); }
          gav = ld128(rs, WS_MODR + (tb * 2304 + (task ? tcol : 0)) * 4); gbv = ld128(rs, WS_MODR + (tb * 2304 + 1728 + (task ? tcol : 0)) * 4);
#pragma unroll
          for (int i = 0; i < 3; i++) bad |= xor4(sbv[i], E) | xor4(cbv[i], E);
          if (all_ok((bad | xor4(gav, E) | xor4(gbv, E)) & 0xffffu)) break;
        }
        cfence();
#pragma unroll
        for (int i = 0; i < 6; i++) { g1p[i] = pk2(1.f + hi2f(cbv[i >> 1][(2 * i) & 3]), 1.f + hi2f(cbv[i >> 1][(2 * i + 1) & 3])); sbp[i] = hi2(sbv[i >> 1][(2 * i) & 3], sbv[i >> 1][(2 * i + 1) & 3]); pin(g1p[i]); pin(sbp[i]); }
        if (sw == 3 && task && lane < 8) { const u32x2 xr = *(const u32x2*)(x + (tb * 576 + tcol) * 2);
          *(LAS u32x4*)(L + L_TP + lane * 48) = gav & 0xffff0000u; *(LAS u32x4*)(L + L_TP + lane * 48 + 16) = gbv & 0xffff0000u;
          *(LAS u32x4*)(L + L_TP + lane * 48 + 32) = u32x4{xr[0] << 16, xr[0] & 0xffff0000u, xr[1] << 16, xr[1] & 0xffff0000u}; }
        if (sw == 2 && lane < 32) *(LAS unsigned*)(L + L_H1 + (lane >> 3) * H1_STR + 96 + (lane & 7) * 4) = 0;
      }
      unsigned o1[3];
#pragma unroll
      for (int i = 0; i < 3; i++) { const int u = 64 * i + lane; o1[i] = (u / 6) * 9216 + (u % 6) * 16; }
      const char* w1b = w1 + slice * 96; const char* w2b = w2 + (size_t)slice * 55296;
      _Pragma("unroll 1") for (int gi = sw; gi < 18; gi += 4) {
#pragma unroll
        for (int i = 0; i < 3; i++) { dma1<WNT>(w1b + (size_t)gi * 32 * 9216, o1[i], L0 + L_W1 + gi * 3072 + i * 1024); waitvm<W1W>(); }
      }
      _Pragma("unroll 1") for (int gi = 32 + sw; gi < 54; gi += 4) { dma1<WNT>(w2b + gi * 1024, lane * 16, L0 + L_W2 + gi * 1024); waitvm<2>(); }
      waitvm<0>();
      STAMP(14);
      lds_wait<2>(L, 7, 4);
      _Pragma("unroll 1") for (int gi = sw; gi < 32; gi += 4) { dma1<WNT>(w2b + gi * 1024, lane * 16, L0 + L_W2 + gi * 1024); waitvm<W2W>(); }
    }
    STAMP(6);
  }
  const int n4 = n >> 2;
  unsigned obase = (n & 3) * (4096 + 64) + OSLOT * 16384 + g * 16;
  int p1off = WS_P1 + ((b * 32 + kvh * 2 + half) * 576 + ((n & 1) ? 448 + 16 * w : 64 * w + 16 * n4) + 4 * g) * 4;
  if (!((n & 3) == 0 || (n == 1 && w >= 4))) p1off = WS_END;
  unsigned E10 = E & 0x3ffu;
  pin(obase); pin(p1off); pin(E10);
  wg_barrier();
  STAMP(7);
  if (w == 3 && lane < 2) st32(rs, WS_FP + (b * 2 + lane) * 128 + (kvh * 2 + half) * 4, E);
  {
    f32x4 d[4] = {{0, 0, 0, 0}, {0, 0, 0, 0}, {0, 0, 0, 0}, {0, 0, 0, 0}}, dx = {0, 0, 0, 0};
    LAS char* ob = L + obase;
    bf16x8 of[8];
#pragma unroll
    for (int s = 0; s < 8; s++) of[s] = *(LAS bf16x8*)(ob + s * 64);
#pragma unroll
    for (int s = 0; s < 8; s++)
#pragma unroll
      for (int u = 0; u < 4; u++) d[u] = mfma32(asbf8(wof[u][s]), of[s], d[u]);
    if (w >= 4) {
#pragma unroll
      for (int s = 0; s < 8; s++) dx = mfma32(asbf8(wox[s]), of[s], dx);
    }
    u32x4 y;
#pragma unroll
    for (int r = 0; r < 4; r++) {
      const float a = sel4(d[0][r], d[1][r], d[2][r], d[3][r]);
      const float ta = a + dppf<XOR1>(a), tb = dx[r] + dppf<XOR1>(dx[r]);
      float t = (n & 1) ? tb : ta; t += dppf<XOR2>(t);
      y[r] = tag10(t, E10);
    }
    st128(rs, p1off, y);
  }
  cfence();
  STAMP(8);
  if (idx < 24) {
    u32x4 w2a[5], w2b[5];
    auto w2read = [&]() {
      LAS char* pa = L + L_W2 + (8 * g + (n >> 2)) * 1152 + (n & 3) * 8;
      LAS char* pb = L + L_W2 + (32 + 8 * (g & 1) + (n >> 2)) * 1152 + (n & 3) * 8;
#pragma unroll
      for (int u = 0; u < 5; u++) { const int o = u < 4 ? w * 128 + u * 32 : 1024 + (w & 3) * 32;
        const u32x2 a = trread(pa + o), c2 = trread(pa + o + 4608); w2a[u] = u32x4{a[0], a[1], c2[0], c2[1]};
        const u32x2 a2 = trread(pb + o), c3 = trread(pb + o + 4608); w2b[u] = u32x4{a2[0], a2[1], c3[0], c3[1]}; } };
    f32x4 x1v, gbv;
    u32x4 w1f[18];
    if (w == 3) {
      if (task) {
        const f32x4 gav = *(LAS f32x4*)(L + L_TP + (lane & 7) * 48), xv = *(LAS f32x4*)(L + L_TP + (lane & 7) * 48 + 32);
        gbv = *(LAS f32x4*)(L + L_TP + (lane & 7) * 48 + 16);
        int po = WS_P1 + ((tb * 32 + p8) * 576 + tcol) * 4, xo = WS_X1 + p8 * X1_REP + (tb * 576 + tcol) * 4;
        pin(po); pin(xo);
        hint_wait(rs, WS_FP + (tb * 2 + (xcd & 1)) * 128 + (lane & 31) * 4, E);
        STAMP(5);
        if (DLY_B) __builtin_amdgcn_s_sleep(DLY_B);
        u32x4 p[4];
        _Pragma("unroll 1") for (unsigned s = 0; s < SPIN_CAP; s++) {
          cfence();
          unsigned bad = 0;
#pragma unroll
          for (int i = 0; i < 4; i++) p[i] = __builtin_amdgcn_raw_buffer_load_b128(rs, po, i * 8 * 2304, 16);
#pragma unroll
          for (int i = 0; i < 4; i++) bad |= xor4(p[i], E);
          if (all_ok(bad & 0x3ffu)) break;
        }
        cfence();
        STAMP(6);
        u32x4 y;
#pragma unroll
        for (int r = 0; r < 4; r++) {
          float s = (un10(p[0][r]) + un10(p[1][r])) + (un10(p[2][r]) + un10(p[3][r]));
          s += dppf<ROR8>(s); s = x16sum(s); s = x32sum(s);
          y[r] = tag16(xv[r] + rbf(gav[r] * rbf(s)), E); x1v[r] = hi2f(y[r]);
        }
        st128(rs, xo, y);
        cfence();
        STAMP(9);
      }
    } else if (w >= 4) {
      int xo = WS_X1 + xcd * X1_REP + ((grp * 4 + w - 4) * 576 + 4 * (lane < 48 ? lane : 47)) * 4;
      unsigned ha = L_H2 + (w - 4) * H2_STR + lane * 8;
      float g1[12], sb[12];
#pragma unroll
      for (int i = 0; i < 12; i++) { g1[i] = (i & 1) ? hi2f(g1p[i >> 1]) : bf2f(g1p[i >> 1]); sb[i] = (i & 1) ? hi2f(sbp[i >> 1]) : bf2f(sbp[i >> 1]); pin(g1[i]); pin(sb[i]); }
      pin(xo); pin(ha);
      u32x4 xv[3];
      _Pragma("unroll 1") for (unsigned s = 0; s < SPIN_CAP; s++) {
        cfence(); unsigned bad = 0;
#pragma unroll
        for (int i = 0; i < 3; i++) xv[i] = ld128(rs, xo + 768 * i);
#pragma unroll
        for (int i = 0; i < 3; i++) bad |= xor4(xv[i], E);
        if (all_ok(bad & 0xffffu)) break;
      }
      cfence();
      STAMP(10);
      const float x0 = hi2f(__builtin_amdgcn_readfirstlane(xv[0][0]));
      float xf[12], s1 = 0, s2 = 0;
#pragma unroll
      for (int i = 0; i < 12; i++) { xf[i] = hi2f(xv[i >> 2][i & 3]); const float dd = xf[i] - x0; s1 += dd; s2 += dd * dd; }
      if (lane >= 48) { s1 = 0; s2 = 0; }
      s1 += dppf<XOR1>(s1); s2 += dppf<XOR1>(s2); s1 += dppf<XOR2>(s1); s2 += dppf<XOR2>(s2);
      s1 += dppf<ROR4>(s1); s2 += dppf<ROR4>(s2); s1 += dppf<ROR8>(s1); s2 += dppf<ROR8>(s2);
      s1 = x16sum(s1); s2 = x16sum(s2); s1 = x32sum(s1); s2 = x32sum(s2);
      const float md = s1 * (1.f / 576.f), var = fmaxf(s2 * (1.f / 576.f) - md * md, 0.f);
      const float mb = rbf(x0 + md), rs_ = __builtin_amdgcn_rcpf(rbf(__builtin_amdgcn_sqrtf(rbf(rbf(var) + 1e-5f))));
      if (lane < 48) {
#pragma unroll
        for (int i = 0; i < 3; i++) { float hh[4];
#pragma unroll
          for (int j = 0; j < 4; j++) hh[j] = (xf[4 * i + j] - mb) * rs_ * g1[4 * i + j] + sb[4 * i + j];
          *(LAS u32x2*)(L + ha + 384 * i) = u32x2{pk2(hh[0], hh[1]), pk2(hh[2], hh[3])}; }
      }
    } else {
      LAS char* p = L + L_W1 + (8 * g + (n >> 2)) * 96 + (n & 3) * 8 + w * 32;
#pragma unroll
      for (int s = 0; s < 18; s++) { const u32x2 a = trread(p + s * 3072), c2 = trread(p + s * 3072 + 384); w1f[s] = u32x4{a[0], a[1], c2[0], c2[1]}; }
    }
    int po = WS_P2 + (((grp * 4 + (n & 3)) * 96 + slice) * 576 + 4 * g) * 4, po2 = n < 4 ? po + (448 + 16 * w) * 4 : WS_END;
    po += (64 * w + 16 * n4) * 4;
    int fo = WS_P2 + ((tb * 96 + 24 * w + p8) * 576 + tcol) * 4;
    unsigned h1a = L_H1 + (n & 3) * H1_STR + g * 16, h2a = L_H2 + (n & 3) * H2_STR + g * 16, h1w = L_H1 + n * H1_STR + (w * 16 + 4 * g) * 2;
    pin(po); pin(po2); pin(h1a); pin(h2a); pin(h1w); pin(fo);
    waitvm<0>();
    wg_barrier();
    STAMP(11);
    if (w >= 3) w2read();
    else {
      f32x4 d = {0, 0, 0, 0}; bf16x8 hf[18];
#pragma unroll
      for (int s = 0; s < 18; s++) hf[s] = *(LAS bf16x8*)(L + h2a + s * 64);
      cfence();
      w2read();
#pragma unroll
      for (int s = 0; s < 18; s++) d = mfma32(asbf8(w1f[s]), hf[s], d);
#pragma unroll
      for (int r = 0; r < 4; r++) d[r] = d[r] < 0.f ? 0.f : d[r];
      if (n < 4) *(LAS u32x2*)(L + h1w) = u32x2{pk2(d[0], d[1]), pk2(d[2], d[3])};
    }
    wg_barrier();
    STAMP(12);
    {
      const bf16x8 ha = *(LAS bf16x8*)(L + h1a), hb = *(LAS bf16x8*)(L + h1a + 64);
      f32x4 d[5];
#pragma unroll
      for (int u = 0; u < 4; u++) d[u] = mfma32(asbf8(w2b[u]), hb, mfma32(asbf8(w2a[u]), ha, f32x4{0, 0, 0, 0}));
      if (w >= 4) d[4] = mfma32(asbf8(w2b[4]), hb, mfma32(asbf8(w2a[4]), ha, f32x4{0, 0, 0, 0}));
      u32x4 y;
#pragma unroll
      for (int r = 0; r < 4; r++) y[r] = tag10(sel4(d[0][r], d[1][r], d[2][r], d[3][r]), E10);
      st128(rs, po, y);
      if (w >= 4) st128(rs, po2, u32x4{tag10(d[4][0], E10), tag10(d[4][1], E10), tag10(d[4][2], E10), tag10(d[4][3], E10)});
    }
    cfence();
    if (task && w < 4) {
      STAMP(13);
      __builtin_amdgcn_s_sleep(DLY_C);
      u32x4 p[3];
      _Pragma("unroll 1") for (unsigned s = 0; s < SPIN_CAP; s++) {
        cfence(); unsigned bad = 0;
#pragma unroll
        for (int i = 0; i < 3; i++) p[i] = __builtin_amdgcn_raw_buffer_load_b128(rs, fo, i * 8 * 2304, 16);
#pragma unroll
        for (int i = 0; i < 3; i++) bad |= xor4(p[i], E);
        if (all_ok(bad & 0x3ffu)) break;
      }
      cfence();
      STAMP(14);
      f32x4 s;
#pragma unroll
      for (int r = 0; r < 4; r++) s[r] = (un10(p[0][r]) + un10(p[1][r])) + un10(p[2][r]);
      if (w < 3) { *(LAS f32x4*)(L + L_W1 + w * 1024 + lane * 16) = s; lds_arrive(L, 6); }
      else {
        lds_wait<0>(L, 6, 3);
        s = (*(LAS f32x4*)(L + L_W1 + lane * 16) + *(LAS f32x4*)(L + L_W1 + 1024 + lane * 16)) + (*(LAS f32x4*)(L + L_W1 + 2048 + lane * 16) + s);
        float o[4];
#pragma unroll
        for (int r = 0; r < 4; r++) { float t = s[r]; t += dppf<ROR8>(t); t = x16sum(t); t = x32sum(t); o[r] = x1v[r] + rbf(gbv[r] * rbf(t)); }
        if (lane < 8) *(u32x2*)(out + (tb * 576 + tcol) * 2) = u32x2{pk2(o[0], o[1]), pk2(o[2], o[3])};
      }
    }
  } else if (bid == 248 && w == 1) {
    _Pragma("unroll 1") for (unsigned s = 0; s < SPIN_CAP; s++) {
      cfence();
      const u32x4 v = ld128(rs, WS_X1 + (lane & 7) * 576 * 4);
      if (all_ok(xor4(v, E) & 0xffffu)) break;
      __builtin_amdgcn_s_sleep(4);
    }
    cfence();
    if (lane == 0) st32(rs, WS_EPOCH, E);
  }
  STAMP(15);
  if (w == 0 && bid == 0 && lane == 0) *(unsigned*)(ws + (8 << 20)) = pt0;
}

__global__ void tinyA(char* ws) { if (threadIdx.x == 0) *(unsigned*)(ws + (8 << 20)) = (unsigned)__builtin_amdgcn_s_memrealtime(); }
__global__ void tinyB(char* ws) { const unsigned tend = *(volatile unsigned*)(ws + (8 << 20)) + 10000; _Pragma("unroll 1") while ((int)((unsigned)__builtin_amdgcn_s_memrealtime() - tend) < 0) __builtin_amdgcn_s_sleep(1); }
extern "C" void kernel_launch(void* const* d_in, const int* in_sizes, int n_in, void* d_out, int out_size, void* d_ws, size_t ws_size, hipStream_t stream) {
  fused_block<<<256, 512, 0, stream>>>((const char*)d_in[1], (const char*)d_in[2], (const char*)d_in[0], (const char*)d_in[3], (const char*)d_in[4], (const char*)d_in[6],
                                       (const char*)d_in[7], (const char*)d_in[8], (const char*)d_in[9], (char*)d_out, (char*)d_ws);
  tinyB<<<1, 64, 0, stream>>>((char*)d_ws);
}
```

```cpp
#include <hip/hip_runtime.h>
#include <hip/hip_bf16.h>
#include <stdint.h>
typedef unsigned short u16;
typedef unsigned long long u64;
typedef short s16x4 __attribute__((ext_vector_type(4)));
typedef __bf16 bf16x8 __attribute__((ext_vector_type(8)));
typedef __bf16 bf16x2 __attribute__((ext_vector_type(2)));
typedef float f32x4 __attribute__((ext_vector_type(4)));
typedef float f32x2 __attribute__((ext_vector_type(2)));
typedef unsigned u32x4 __attribute__((ext_vector_type(4)));
typedef unsigned u32x2 __attribute__((ext_vector_type(2)));
#define LAS __attribute__((address_space(3)))
#define DEVI __device__ __forceinline__
#define STAMPS 0

constexpr int WS_EPOCH = 0;
constexpr int WS_FT = 1024;
constexpr int WS_HG = 4096, HG_REP = 18 * 2048;
constexpr int WS_QG = WS_HG + 8 * HG_REP, QG_PL = 8 * 4096 * 4;
constexpr int WS_MODR = WS_QG + 6 * QG_PL;
constexpr int WS_ML = WS_MODR + 8 * 2304 * 4;
constexpr int WS_OG = WS_ML + 256 * 128;
constexpr int WS_P1 = WS_OG + 256 * 1024;
constexpr int WS_X1 = WS_P1 + 8 * 16 * 576 * 4, X1_REP = 8 * 576 * 4;
constexpr int WS_P2 = WS_X1 + 8 * X1_REP;
constexpr int WS_END = WS_P2 + 8 * 72 * 576 * 4;
constexpr int WS_STAMP = 4 << 20;
constexpr int L_W2 = 49152, L_W1 = L_W2 + 36864;
constexpr int L_RED = 131072, L_NX = L_RED + 17408;
constexpr int L_WO = 49152, L_OB = L_WO + 98304, OB_STR = 528;
constexpr int L_MISC = 159744;
constexpr int L_CNT = L_MISC, L_ML = L_MISC + 64, L_SC = L_MISC + 128, L_Q = L_MISC + 256, L_H1 = L_MISC + 1024, L_TP = L_MISC + 2048;
constexpr int H2_STR = 1216, H1_STR = 144;
constexpr int L_TOTAL = 163840;
constexpr unsigned SPIN_CAP = 400000;

DEVI float bf2f(unsigned v) { return __uint_as_float(v << 16); }
DEVI float hi2f(unsigned v) { return __uint_as_float(v & 0xffff0000u); }
DEVI unsigned pk2(float lo, float hi) { bf16x2 v = {(__bf16)lo, (__bf16)hi}; return __builtin_bit_cast(unsigned, v); }
DEVI float rbf(float f) { return hi2f(pk2(f, f)); }
template <int CTRL> DEVI unsigned dppu(unsigned x) { return (unsigned)__builtin_amdgcn_mov_dpp((int)x, CTRL, 0xf, 0xf, true); }
template <int CTRL> DEVI float dppf(float x) { return __uint_as_float(dppu<CTRL>(__float_as_uint(x))); }
constexpr int XOR1 = 0xB1, XOR2 = 0x4E, HMIR = 0x141, ROR4 = 0x124, ROR8 = 0x128;
DEVI float x16sum(float x) { auto s = __builtin_amdgcn_permlane16_swap(__float_as_uint(x), __float_as_uint(x), false, false); return __uint_as_float(s[0]) + __uint_as_float(s[1]); }
DEVI float x32sum(float x) { auto s = __builtin_amdgcn_permlane32_swap(__float_as_uint(x), __float_as_uint(x), false, false); return __uint_as_float(s[0]) + __uint_as_float(s[1]); }
DEVI float x16max(float x) { auto s = __builtin_amdgcn_permlane16_swap(__float_as_uint(x), __float_as_uint(x), false, false); return fmaxf(__uint_as_float(s[0]), __uint_as_float(s[1])); }
DEVI float x32max(float x) { auto s = __builtin_amdgcn_permlane32_swap(__float_as_uint(x), __float_as_uint(x), false, false); return fmaxf(__uint_as_float(s[0]), __uint_as_float(s[1])); }
DEVI unsigned lo2(unsigned a, unsigned b) { return __builtin_amdgcn_perm(b, a, 0x05040100u); }
DEVI unsigned hi2(unsigned a, unsigned b) { return __builtin_amdgcn_perm(b, a, 0x07060302u); }
DEVI bf16x8 asbf8(u32x4 v) { return __builtin_bit_cast(bf16x8, v); }
DEVI f32x4 mfma32(bf16x8 a, bf16x8 b, f32x4 c) { return __builtin_amdgcn_mfma_f32_16x16x32_bf16(a, b, c, 0, 0, 0); }
DEVI f32x4 mfma16(u32x2 a, u32x2 b, f32x4 c) { return __builtin_amdgcn_mfma_f32_16x16x16bf16_1k(__builtin_bit_cast(s16x4, a), __builtin_bit_cast(s16x4, b), c, 0, 0, 0); }
DEVI u32x2 trread(LAS char* p) { return __builtin_bit_cast(u32x2, __builtin_amdgcn_ds_read_tr16_b64_v4i16((LAS s16x4*)p)); }
DEVI void cfence() { asm volatile("" ::: "memory"); }
DEVI void wg_barrier() { asm volatile("s_waitcnt lgkmcnt(0)\n\ts_barrier" ::: "memory"); }
template <int N> DEVI void waitvm() { asm volatile("s_waitcnt vmcnt(%0)" ::"i"(N) : "memory"); }
DEVI void waitlgkm() { asm volatile("s_waitcnt lgkmcnt(0)" ::: "memory"); }
typedef __amdgpu_buffer_rsrc_t rsrc_t;
DEVI u32x4 ld128(rsrc_t rs, int off) { return __builtin_amdgcn_raw_buffer_load_b128(rs, off, 0, 16); }
DEVI void st128(rsrc_t rs, int off, u32x4 v) { __builtin_amdgcn_raw_buffer_store_b128(v, rs, off, 0, 16); }
DEVI rsrc_t mkrs(const char* p, int bytes) { return __builtin_amdgcn_make_buffer_rsrc((void*)p, 0, bytes, 0x00020000); }
template <int BANK> DEVI float banksel(float old, float v) { return __uint_as_float((unsigned)__builtin_amdgcn_update_dpp((int)__float_as_uint(old), (int)__float_as_uint(v), 0xE4, 0xf, 1 << BANK, false)); }
DEVI float sel4(float a, float b, float c, float d) { return banksel<3>(banksel<2>(banksel<1>(a, b), c), d); }
template <class T> DEVI void pin(T& v) { asm volatile("" : "+v"(v)); }
DEVI unsigned ld32(rsrc_t rs, int off) { return __builtin_amdgcn_raw_buffer_load_b32(rs, off, 0, 16); }
DEVI void st32(rsrc_t rs, int off, unsigned v) { __builtin_amdgcn_raw_buffer_store_b32(v, rs, off, 0, 16); }
DEVI unsigned xor4(u32x4 v, unsigned t) { return (v[0] ^ t) | (v[1] ^ t) | (v[2] ^ t) | (v[3] ^ t); }
DEVI bool all_ok(unsigned bad) { return __builtin_amdgcn_ballot_w64(bad != 0) == 0; }
DEVI unsigned tag16(float v, unsigned t) { return (pk2(v, v) & 0xffff0000u) | (t & 0xffffu); }
DEVI unsigned tag10(float v, unsigned t10) { return (__float_as_uint(v) & ~0x3ffu) | t10; }
DEVI float un10(unsigned v) { return __uint_as_float(v & ~0x3ffu); }
DEVI unsigned now() { return (unsigned)__builtin_amdgcn_s_memrealtime(); }
DEVI void until(unsigned t) {
  _Pragma("unroll 1") while ((int)(now() - t) < -50) __builtin_amdgcn_s_sleep(10);
  _Pragma("unroll 1") while ((int)(now() - t) < 0) {}
  cfence(); }
DEVI void lds_arrive(LAS char* L, int i) { waitlgkm(); if ((threadIdx.x & 63) == 0) __hip_atomic_fetch_add((LAS unsigned*)(L + L_CNT + 4 * i), 1u, __ATOMIC_RELAXED, __HIP_MEMORY_SCOPE_WORKGROUP); cfence(); }
template <int SLEEP> DEVI void lds_wait(LAS char* L, int i, unsigned target) {
  _Pragma("unroll 1") for (unsigned s = 0; s < 16 * SPIN_CAP; s++) {
    unsigned v = *(volatile LAS unsigned*)(L + L_CNT + 4 * i);
    if (__builtin_amdgcn_readfirstlane(v) >= target) break;
    if (SLEEP) __builtin_amdgcn_s_sleep(SLEEP);
  }
  cfence();
}
#define DMA4_BODY(POL) asm volatile("s_mov_b32 %0, m0\n\ts_mov_b32 m0, %6\n\ts_nop 0\n\t" \
     "global_load_lds_dwordx4 %1, %5 offset:0" POL "\n\tglobal_load_lds_dwordx4 %2, %5 offset:1024" POL "\n\t" \
     "global_load_lds_dwordx4 %3, %5 offset:2048" POL "\n\tglobal_load_lds_dwordx4 %4, %5 offset:3072" POL "\n\ts_mov_b32 m0, %0" \
     : "=&s"(keep) : "v"(o[0]), "v"(o[1]), "v"(o[2]), "v"(o[3]), "s"(sbase), "s"(dst) : "memory")
template <int NT> DEVI void dma4(const char* sbase, const unsigned (&o)[4], unsigned dst) { unsigned keep; if (NT) DMA4_BODY(" nt"); else DMA4_BODY(""); }
template <int NT> DEVI void dma1(const char* sbase, unsigned o, unsigned dst) {
  unsigned keep;
  if (NT) asm volatile("s_mov_b32 %0, m0\n\ts_mov_b32 m0, %3\n\ts_nop 0\n\tglobal_load_lds_dwordx4 %1, %2 nt\n\ts_mov_b32 m0, %0" : "=&s"(keep) : "v"(o), "s"(sbase), "s"(dst) : "memory");
  else asm volatile("s_mov_b32 %0, m0\n\ts_mov_b32 m0, %3\n\ts_nop 0\n\tglobal_load_lds_dwordx4 %1, %2\n\ts_mov_b32 m0, %0" : "=&s"(keep) : "v"(o), "s"(sbase), "s"(dst) : "memory");
}
#define WNT 0
template <int R> DEVI unsigned ror(unsigned x) { if constexpr (R == 0) return x; else return dppu<0x120 + R>(x); }
template <int T8, int U> DEVI void pv1(f32x4& acc, unsigned z0, unsigned z1, u32x2 v) {
  constexpr int R = (2 * (U - T8) + 16) & 15;
  acc = mfma16(v, u32x2{ror<R>(z0), ror<R>(z1)}, acc);
}
#define NPRE 12
constexpr int NF = 8 + NPRE;
constexpr int slot_of(int t) { return t < NF ? (t + 16 - NPRE) % 8 : (t - NF) % 3; }
template <int WT> DEVI void dma4_paced(const char* sbase, const unsigned (&o)[4], unsigned dst) { unsigned keep;
  asm volatile("s_mov_b32 %0, m0\n\ts_mov_b32 m0, %6\n\ts_waitcnt vmcnt(%7) lgkmcnt(0)\n\t"
     "global_load_lds_dwordx4 %1, %5 offset:0 nt\n\ts_waitcnt vmcnt(%7)\n\tglobal_load_lds_dwordx4 %2, %5 offset:1024 nt\n\ts_waitcnt vmcnt(%7)\n\t"
     "global_load_lds_dwordx4 %3, %5 offset:2048 nt\n\ts_waitcnt vmcnt(%7)\n\tglobal_load_lds_dwordx4 %4, %5 offset:3072 nt\n\ts_mov_b32 m0, %0"
     : "=&s"(keep) : "v"(o[0]), "v"(o[1]), "v"(o[2]), "v"(o[3]), "s"(sbase), "s"(dst), "i"(WT) : "memory"); }
#define W1W 1
#define DEEP 4
#define PRIO 2
#define NE2 0
#define TLATE 60
#define FO -10
#define SO 8
#define FB 135
#define SB 36
#define FX 0
#define SX 12
#define SC 21
#define FC 0
#define WARM 3
#define TWARM 3700
#define GREC 150
#define GL 30
#define TEND 160
#define TW1 34
#define WL2 6
#define TH 480
#define TQ 850
#define GOFF 160
#define GTAU 612
#define PW 0
constexpr int OSLOT = slot_of(62), L_H2 = slot_of(63) * 16384;
static_assert(NPRE >= 6 && NPRE <= 12 && NE2 <= 36 && TW1 <= TLATE, "schedule");
template <int I, int N, class F> DEVI void sfor(F&& f) { if constexpr (I < N) { f(std::integral_constant<int, I>{}); sfor<I + 1, N>(f); } }
#define STAMP(i) do { if ((i) == 0) { pt0 = (unsigned)__builtin_amdgcn_s_memrealtime(); ptp = pt0 + (0); } else if ((i) == 4) ptp = (unsigned)__builtin_amdgcn_s_memrealtime(); } while (0)

extern "C" __global__ void __launch_bounds__(512, 2) fused_block(const char* Kc, const char* Vc, const char* x, const char* wq, const char* wo, const char* w1,
                                                      const char* w2, const char* cc, const char* wm, char* out, char* ws) {
  __shared__ __attribute__((aligned(16))) char lds_mem[L_TOTAL];
  LAS char* L = (LAS char*)lds_mem;
  const unsigned L0 = (unsigned)(size_t)L;
  const int lane = threadIdx.x & 63, w = __builtin_amdgcn_readfirstlane(threadIdx.x >> 6);
  const int bid = blockIdx.x, xcd = bid & 7, idx = bid >> 3;
  const int kvh = 2 * xcd + (idx & 1), b = (idx >> 1) & 7, half = idx >> 4;
  const int n = lane & 15, g = lane >> 4;
  const rsrc_t rs = __builtin_amdgcn_make_buffer_rsrc((void*)ws, 0, WS_END, 0x00020000);
  unsigned pt0, ptp;
  STAMP(0);
  const unsigned tstart = now();
  if (threadIdx.x < 12) *(LAS unsigned*)(L + L_CNT + threadIdx.x * 4) = 0;
  wg_barrier();
  u32x4 wof[2][8];
  unsigned E;
  unsigned g1p[6], sbp[6];
  const int i12 = idx % 12, grp = idx / 12, slice = 9 * xcd + i12;
  const bool mlp = idx < 24 && i12 < 9, proj = idx < 24 && i12 >= 9;
  const int tb = 4 * grp + (xcd >> 1), tcol = 32 * (9 * (xcd & 1) + i12) + 4 * (lane & 7), p8 = lane >> 3;
  const int j1 = (idx - 24) * 8 + xcd;
  const bool s1 = idx >= 24 && j1 < 54;
  const int pk = 2 * xcd + grp, cs = i12 - 9;
  u32x4 w1f[18];

  if (w < 4) {
    const size_t base = (size_t)(b * 16 + kvh) * 1048576 + (size_t)half * 524288 + w * 4096;
    const char* kb = Kc + base; const char* vb = Vc + base;
    unsigned tg0 = tstart + GOFF + w * (GTAU / 40);
    auto gate = [&](int t) {
      if (GTAU) { _Pragma("unroll 1") while ((int)((unsigned)__builtin_amdgcn_s_memrealtime() - tg0 - t * GTAU / 10) < 0) {} } };
    unsigned ko[4], vo[4];
#pragma unroll
    for (int i = 0; i < 4; i++) { const int row = 4 * i + g; ko[i] = g * 256 + ((n ^ row) * 16); vo[i] = g * 256 + ((n ^ ((row & 7) << 1)) * 16); }
    const unsigned ring0 = L0 + w * 4096;
    LAS char* ring = L + w * 4096;
    unsigned kr[4];
#pragma unroll
    for (int s = 0; s < 4; s++) kr[s] = n * 256 + (((4 * s + g) ^ n) * 16);
    auto issue = [&](auto T) { constexpr int t = T; if constexpr (t < 64) gate(t); waitlgkm();
      if constexpr (t < 32) dma4<1>(kb + t * 16384, ko, ring0 + slot_of(t) * 16384);
      else if constexpr (t < 64) dma4<1>(vb + (t - 32) * 16384, vo, ring0 + slot_of(t) * 16384); };
    bf16x8 pre[NPRE][4];
    lds_wait<0>(L, 4, 4);
    sfor<0, NF>([&](auto T) { constexpr int t = T;
      if constexpr (PW > 0) { gate(t); dma4_paced<PW>(kb + t * 16384, ko, ring0 + slot_of(t) * 16384); }
      else { if constexpr (t >= 3) waitvm<8>(); issue(T); }
      if constexpr (t >= 3 && t - 3 < NPRE) {
#pragma unroll
        for (int s = 0; s < 4; s++) pre[t - 3][s] = *(LAS bf16x8*)(ring + slot_of(t - 3) * 16384 + kr[s]); } });
    const int vrow = 4 * g + (n >> 2), vp = n & 3, n2 = n >> 1;
    const unsigned vr0 = vrow * 256 + (vp >> 1) * 16 + (vp & 1) * 8, vx = (vrow & 7) << 5;
    STAMP(1);
    lds_wait<0>(L, 8, 1);
    STAMP(2);
    bf16x8 qf[4];
    f32x4 S[4] = {{0, 0, 0, 0}, {0, 0, 0, 0}, {0, 0, 0, 0}, {0, 0, 0, 0}};
    auto score = [&](auto T, const bf16x8 (&kf)[4]) { constexpr int t = T;
      f32x4 d = {0, 0, 0, 0};
#pragma unroll
      for (int s = 0; s < 4; s++) d = mfma32(kf[s], qf[s], d);
#pragma unroll
      for (int r = 0; r < 4; r++) S[t >> 3][r] = (n2 == (t & 7)) ? d[r] : S[t >> 3][r]; };
    auto kread = [&](auto T, bf16x8 (&kf)[4]) { constexpr int t = T;
#pragma unroll
      for (int s = 0; s < 4; s++) kf[s] = *(LAS bf16x8*)(ring + slot_of(t) * 16384 + kr[s]); };
    auto wwait = [&](auto T) { constexpr int t = T; waitvm<(t + 1 < 64 ? 4 : 0) + (t + 2 < 64 ? 4 : 0)>(); };
    auto wnext = [&](auto T) { constexpr int t = T;
      if constexpr (DEEP < 4 && t + 3 < 64) waitvm<4 + DEEP>();
      issue(std::integral_constant<int, t + 3>{}); };
#pragma unroll
    for (int s = 0; s < 4; s++) qf[s] = *(LAS bf16x8*)(L + L_Q + (n & 1) * 256 + (4 * s + g) * 16);
    E = *(LAS unsigned*)(L + L_CNT + 60);
    tg0 = __builtin_amdgcn_readfirstlane(*(LAS unsigned*)(L + L_CNT + 56)) + GOFF + w * (GTAU / 40);
    sfor<NPRE, NPRE + 3>([&](auto T) { constexpr int t = T, i = t - NPRE;
      bf16x8 kf[4]; kread(T, kf); score(T, kf);
      if constexpr (i > 0) sfor<(i - 1) * NPRE / 2, i * NPRE / 2>([&](auto P) { score(P, pre[P]); });
      issue(std::integral_constant<int, NF + i>{}); });
    sfor<NPRE + 3, 32>([&](auto T) { constexpr int t = T;
      if constexpr (t >= NF) wwait(T); else if constexpr (t >= NF - 3) waitvm<4 * (NF + 2 - t)>();
      bf16x8 kf[4]; kread(T, kf);
      if constexpr (t >= NF) wnext(T);
      score(T, kf);
      if constexpr (t == NF - 1) lds_arrive(L, 5);
    });
    STAMP(3);
    float m = -INFINITY;
#pragma unroll
    for (int G = 0; G < 4; G++)
#pragma unroll
      for (int r = 0; r < 4; r++) { const float s = rbf(rbf(S[G][r]) * 0.08838834764831845f); S[G][r] = s; m = fmaxf(m, s); }
    m = fmaxf(m, dppf<XOR2>(m)); m = fmaxf(m, dppf<ROR4>(m)); m = fmaxf(m, dppf<ROR8>(m)); m = x16max(m); m = x32max(m);
    const float msub = (m == -INFINITY) ? 0.f : m;
    float l = 0; unsigned P[4][2];
#pragma unroll
    for (int G = 0; G < 4; G++) { float e[4];
#pragma unroll
      for (int r = 0; r < 4; r++) { e[r] = __builtin_amdgcn_exp2f((S[G][r] - msub) * 1.4426950408889634f); l += e[r]; }
      P[G][0] = pk2(e[0], e[1]); P[G][1] = pk2(e[2], e[3]); }
    l += dppf<XOR2>(l); l += dppf<ROR4>(l); l += dppf<ROR8>(l); l = x16sum(l); l = x32sum(l);
    if (lane < 2) *(LAS f32x2*)(L + L_ML + w * 16 + lane * 8) = f32x2{m, l};
    lds_arrive(L, 2);
    f32x4 acc = {0, 0, 0, 0};
    float sc; unsigned oaddr, oown; int goff;
    sfor<32, 64>([&](auto T) { constexpr int t = T; constexpr int T8 = t & 7, G = (t - 32) >> 3;
      if constexpr (t == 63) pin(acc);
      wwait(T);
      u32x2 v[8];
#pragma unroll
      for (int u = 0; u < 8; u++) v[u] = trread(ring + slot_of(t) * 16384 + vr0 + ((u << 5) ^ vx));
      wnext(T);
      const unsigned z0 = (n2 == T8) ? P[G][0] : 0u, z1 = (n2 == T8) ? P[G][1] : 0u;
      sfor<0, 8>([&](auto U) { pv1<T8, U>(acc, z0, z1, v[U]); });
      if constexpr (t == 56) { lds_wait<1>(L, 3, 1); sc = *(LAS float*)(L + L_SC + w * 8 + (n & 1) * 4);
        oaddr = L0 + OSLOT * 16384 + lane * 16; oown = oaddr + w * 4096; pin(oown); goff = WS_OG + ((kvh * 8 + b) * 2 + half) * 1024 + ((n & 1) * 128 + n2 * 16 + g * 4) * 4; pin(sc); pin(oaddr); pin(goff); }
      if constexpr (t == TW1) { if (mlp) { lds_wait<1>(L, 10, 4);
        LAS char* p = L + L_W1 + (8 * g + (n >> 2)) * 128 + (n & 3) * 8 + w * 32;
#pragma unroll
        for (int s = 0; s < 18; s++) { const u32x2 a = trread(p + s * 4096), c2 = trread(p + s * 4096 + 512); w1f[s] = u32x4{a[0], a[1], c2[0], c2[1]}; pin(w1f[s]); } } }
      if constexpr (t == TLATE) lds_arrive(L, 5);
    });
    STAMP(4);
    f32x4 o0, o1, o2, o3; unsigned tk;
    asm volatile("ds_write_b128 %5, %6\n\ts_mov_b64 exec, 1\n\tds_add_rtn_u32 %4, %7, %8\n\ts_mov_b64 exec, -1\n\t"
                 "ds_read_b128 %0, %9\n\tds_read_b128 %1, %9 offset:4096\n\tds_read_b128 %2, %9 offset:8192\n\tds_read_b128 %3, %9 offset:12288\n\ts_waitcnt lgkmcnt(0)"
                 : "=&v"(o0), "=&v"(o1), "=&v"(o2), "=&v"(o3), "=&v"(tk) : "v"(oown), "v"(acc * sc), "v"(L0 + L_CNT + 4), "v"(1u), "v"(oaddr) : "memory");
    if (__builtin_amdgcn_readfirstlane(tk) == 3) {
      const f32x4 o = (o0 + o1) + (o2 + o3);
      const unsigned t10 = E & 0x3ffu;
      st128(rs, goff, u32x4{tag10(o[0], t10), tag10(o[1], t10), tag10(o[2], t10), tag10(o[3], t10)});
    }
    cfence();
    STAMP(7);
  } else {
    const int sw = w - 4, ks = n >> 3, c8 = n & 7;
    if (PRIO) __builtin_amdgcn_s_setprio(PRIO);
    const unsigned Ev = __builtin_amdgcn_raw_buffer_load_b32(rs, WS_EPOCH, 0, 16);
    auto warm = [&]() {
      u64 pc; unsigned o, sz;
      asm volatile("s_getpc_b64 %0\n.Lwarm%=:\n\ts_mov_b32 %1, .Lwarm%=-fused_block\n\ts_mov_b32 %2, .Lfb_end-fused_block" : "=s"(pc), "=s"(o), "=s"(sz));
      const rsrc_t rc = mkrs((const char*)(pc - o), sz); unsigned v[8];
#pragma unroll
      for (int i = 0; i < 8; i++) v[i] = __builtin_amdgcn_raw_buffer_load_b32(rc, lane * 128 + i * 8192, 0, 0);
      asm volatile("" :: "v"(v[0]), "v"(v[1]), "v"(v[2]), "v"(v[3]), "v"(v[4]), "v"(v[5]), "v"(v[6]), "v"(v[7])); };
    if ((WARM & 1) && idx == 31 && sw == 1) warm();
    u32x4 wr[3][8];
    f32x4 a[8];
#pragma unroll
    for (int e = 0; e < 8; e++) a[e] = f32x4{0, 0, 0, 0};
    auto xpose = [&](u32x4 (&r)[8]) { u32x4 f[8];
#pragma unroll
      for (int e = 0; e < 4; e++)
#pragma unroll
        for (int j = 0; j < 4; j++) { f[2 * e][j] = lo2(r[2 * j][e], r[2 * j + 1][e]); f[2 * e + 1][j] = hi2(r[2 * j][e], r[2 * j + 1][e]); }
#pragma unroll
      for (int e = 0; e < 8; e++) r[e] = f[e]; };
    auto mm8 = [&](u32x4 af, const u32x4 (&f)[8]) {
#pragma unroll
      for (int e = 0; e < 8; e++) a[e] = mfma32(asbf8(af), asbf8(f[e]), a[e]); };
    auto redwrite = [&]() {
      if ((g >> 1) == ks) { LAS char* p = L + L_RED + ((sw * 2 + ks) * 8 + (g & 1) * 4) * 272 + c8 * 32;
#pragma unroll
        for (int r = 0; r < 4; r++) { *(LAS f32x4*)(p + r * 272) = f32x4{a[0][r], a[1][r], a[2][r], a[3][r]}; *(LAS f32x4*)(p + r * 272 + 16) = f32x4{a[4][r], a[5][r], a[6][r], a[7][r]}; } }
      lds_arrive(L, 0); };
    const int krow = 64 * sw + 32 * ks + 8 * g;
    u32x4 ew[24];
    auto early = [&]() {
      if (mlp) { const rsrc_t r1 = mkrs(w1, 576 * 9216); const int o = (8 * sw + (lane >> 3)) * 9216 + slice * 128 + (lane & 7) * 16;
#pragma unroll
        for (int i = 0; i < 18; i++) ew[i] = __builtin_amdgcn_raw_buffer_load_b128(r1, o, i * 32 * 9216, 0);
      } else { const rsrc_t ro = mkrs(wo, 4096 * 1152); int o[3];
#pragma unroll
        for (int i = 0; i < 3; i++) { const int u = 64 * i + lane; o[i] = (pk * 256 + 8 * sw + u / 24) * 1152 + cs * 384 + (u % 24) * 16; }
#pragma unroll
        for (int i = 0; i < 24; i++) ew[i] = __builtin_amdgcn_raw_buffer_load_b128(ro, o[i % 3], (i / 3) * 32 * 1152, 0);
      } };
    if (s1) {
      const rsrc_t rc = mkrs(cc, 9216), rx = mkrs(x, 9216), rm = mkrs(wm, 576 * 6912);
      const int co = (c8 * 576 + krow) * 2, wo_ = krow * 6912 + j1 * 128 + c8 * 16;
      const int fc = 32 * ks + 8 * g + 4 * (sw & 1);
      const bool isca = j1 >= 9 && j1 < 18;
      const int fb = 2 * sw + (g & 1), j24 = (g >> 1) * 16 + n;
      u32x4 cv[3], xv[3];
      cv[0] = __builtin_amdgcn_raw_buffer_load_b128(rc, co, 0, 0); cv[1] = __builtin_amdgcn_raw_buffer_load_b128(rc, co + 512, 0, 0);
      cv[2] = __builtin_amdgcn_raw_buffer_load_b128(rc, co + 1024, 0, 0);
#pragma unroll
      for (int i = 0; i < 3; i++) xv[i] = __builtin_amdgcn_raw_buffer_load_b128(rx, (isca && j24 < 24) ? fb * 1152 + (j24 + 24 * i) * 16 : 9216, 0, 0);
      const u32x2 xs = __builtin_amdgcn_raw_buffer_load_b64(rx, isca ? fb * 1152 + (j1 - 9) * 128 + n * 8 : 9216, 0, 0);
#pragma unroll
      for (int i = 0; i < 2; i++)
#pragma unroll
        for (int j = 0; j < 8; j++) wr[i][j] = __builtin_amdgcn_raw_buffer_load_b128(rm, wo_, (i * 256 + j) * 6912, 0);
      if (sw == 0) {
#pragma unroll
        for (int j = 0; j < 8; j++) wr[2][j] = __builtin_amdgcn_raw_buffer_load_b128(rm, wo_, (512 + j) * 6912, 0);
      }
      __builtin_amdgcn_sched_barrier(0); if (j1 >= 18) lds_arrive(L, 4); __builtin_amdgcn_sched_barrier(0);
      cfence();
      STAMP(13);
      E = __builtin_amdgcn_readfirstlane(Ev) + 1u;
#pragma unroll
      for (int i = 0; i < 3; i++)
#pragma unroll
        for (int j = 0; j < 4; j++) { const float v0 = bf2f(cv[i][j]), v1 = hi2f(cv[i][j]);
          cv[i][j] = pk2(v0 * __builtin_amdgcn_rcpf(1.f + __expf(-v0)), v1 * __builtin_amdgcn_rcpf(1.f + __expf(-v1))); }
      if (isca) {
        float s = 0;
#pragma unroll
        for (int i = 0; i < 12; i++) s += bf2f(xv[i >> 2][i & 3]) + hi2f(xv[i >> 2][i & 3]);
        s += dppf<XOR1>(s); s += dppf<XOR2>(s); s += dppf<ROR4>(s); s += dppf<ROR8>(s); s = x32sum(s);
        const float mean = s * (1.f / 576.f); float v = 0;
#pragma unroll
        for (int i = 0; i < 12; i++) { const float d0 = bf2f(xv[i >> 2][i & 3]) - mean, d1 = hi2f(xv[i >> 2][i & 3]) - mean; v += d0 * d0; v += d1 * d1; }
        if (j24 >= 24) v = 0;
        v += dppf<XOR1>(v); v += dppf<XOR2>(v); v += dppf<ROR4>(v); v += dppf<ROR8>(v); v = x32sum(v);
        const float mb = rbf(mean), sd = rbf(sqrtf(rbf(rbf(v * (1.f / 576.f)) + 1e-5f)));
        f32x4 nx;
#pragma unroll
        for (int r = 0; r < 4; r++) nx[r] = rbf(rbf(((r & 1) ? hi2f(xs[r >> 1]) : bf2f(xs[r >> 1])) - mb) / sd);
        *(LAS f32x4*)(L + L_NX + fb * 272 + n * 16) = nx;
      }
      cfence();
      xpose(wr[0]); mm8(cv[0], wr[0]); xpose(wr[1]); mm8(cv[1], wr[1]);
      if (sw == 0) { xpose(wr[2]); mm8(cv[2], wr[2]); }
      STAMP(1);
      redwrite();
      lds_wait<0>(L, 0, 4);
      const f32x4 nx = *(LAS f32x4*)(L + L_NX + c8 * 272 + fc * 4);
      f32x4 s = {0, 0, 0, 0};
#pragma unroll
      for (int p = 0; p < 8; p++) s += *(LAS f32x4*)(L + L_RED + (p * 8 + c8) * 272 + fc * 4);
      u32x4 y;
#pragma unroll
      for (int r = 0; r < 4; r++) y[r] = tag16(isca ? nx[r] * rbf(1.f + rbf(s[r])) : s[r], E);
      if (j1 < 18) { const int o = WS_HG + (sw >> 1) * 4 * HG_REP + j1 * 2048 + (sw & 1) * 1024 + lane * 16;
#pragma unroll
        for (int i = 0; i < 4; i++) st128(rs, o + i * HG_REP, y);
        lds_arrive(L, 4); }
      else if (sw < 2) st128(rs, WS_MODR + (c8 * 2304 + (j1 - 18) * 64 + fc) * 4, y);
      STAMP(2);
    } else if (idx < 24 && sw < 3) {
      const rsrc_t rq = mkrs(wq, 576 * 8192);
      const int bl = 24 * xcd + idx, k3 = bl >> 6, qc = 64 * (bl & 63), k0 = 192 * k3 + krow;
#pragma unroll
      for (int j = 0; j < 8; j++) wr[0][j] = __builtin_amdgcn_raw_buffer_load_b128(rq, k0 * 8192 + qc * 2 + c8 * 16, j * 8192, 0);
      __builtin_amdgcn_sched_barrier(0); early(); __builtin_amdgcn_sched_barrier(0); lds_arrive(L, 4); __builtin_amdgcn_sched_barrier(0);
      STAMP(13);
      E = __builtin_amdgcn_readfirstlane(Ev) + 1u;
      xpose(wr[0]);
#pragma unroll
      for (int e = 0; e < 8; e++) pin(wr[0][e]);
      STAMP(1);
      const int hoff = WS_HG + xcd * HG_REP + (3 * k3 + sw) * 2048 + lane * 16;
      until(tstart + TH);
      u32x4 h[4];
      _Pragma("unroll 1") for (unsigned s = 0; s < SPIN_CAP; s++) {
        cfence();
        h[0] = ld128(rs, hoff); h[1] = ld128(rs, hoff + 1024); h[2] = ld128(rs, hoff + 9 * 2048); h[3] = ld128(rs, hoff + 9 * 2048 + 1024);
        if (all_ok((xor4(h[0], E) | xor4(h[1], E) | xor4(h[2], E) | xor4(h[3], E)) & 0xffffu)) break;
      }
      cfence();
      STAMP(3);
      u32x4 af;
#pragma unroll
      for (int j = 0; j < 4; j++) af[j] = pk2(hi2f(h[j >> 1][(2 * j) & 3]) + hi2f(h[2 + (j >> 1)][(2 * j) & 3]), hi2f(h[j >> 1][(2 * j + 1) & 3]) + hi2f(h[2 + (j >> 1)][(2 * j + 1) & 3]));
      mm8(af, wr[0]);
      redwrite();
      lds_wait<0>(L, 0, 3);
      if (sw < 2) {
        f32x4 s = {0, 0, 0, 0};
#pragma unroll
        for (int p = 0; p < 6; p++) s += *(LAS f32x4*)(L + L_RED + (p * 8 + 4 * sw + g) * 272 + n * 16);
        u32x4 yh, yl;
#pragma unroll
        for (int r = 0; r < 4; r++) { const unsigned u = __float_as_uint(s[r]); yh[r] = (u & 0xffff0000u) | (E & 0xffffu); yl[r] = (u << 16) | (E & 0xffffu); }
        const int o = WS_QG + 2 * k3 * QG_PL + ((4 * sw + g) * 4096 + qc + 4 * n) * 4;
        st128(rs, o, yh); st128(rs, o + QG_PL, yl);
      }
      STAMP(2);
    } else {
      if (idx < 24) {
        const rsrc_t rm = mkrs(wm, 576 * 6912);
        const int l24 = lane >= 24 ? 1 : 0; unsigned pf[4];
#pragma unroll
        for (int i = 0; i < 4; i++) { const int j = xcd + 8 * (2 * i + l24);
          pf[i] = __builtin_amdgcn_raw_buffer_load_b32(rm, (lane < 48 && j < 54) ? (24 * idx + lane - 24 * l24) * 6912 + j * 128 : 576 * 6912, 0, 0); }
        __builtin_amdgcn_sched_barrier(0); early(); __builtin_amdgcn_sched_barrier(0); lds_arrive(L, 4);
        asm volatile("" :: "v"(pf[0]), "v"(pf[1]), "v"(pf[2]), "v"(pf[3]));
      } else lds_arrive(L, 4);
      E = __builtin_amdgcn_readfirstlane(Ev) + 1u;
      if (bid < 8 && lane < 8) st32(rs, WS_FT + lane * 128 + bid * 4, (tstart << 16) | (E & 0xffffu));
    }
    lds_arrive(L, 9);
    if (sw == 3) {
      u32x4 qv[6]; unsigned tc;
      until(tstart + TQ);
      _Pragma("unroll 1") for (unsigned s = 0; s < SPIN_CAP; s++) {
        cfence(); unsigned bad = 0;
#pragma unroll
        for (int i = 0; i < 6; i++) qv[i] = ld128(rs, WS_QG + i * QG_PL + (b * 4096 + kvh * 256 + 4 * lane) * 4);
        tc = ld32(rs, WS_FT + xcd * 128 + (lane & 7) * 4);
#pragma unroll
        for (int i = 0; i < 6; i++) bad |= xor4(qv[i], E);
        if (all_ok(bad & 0xffffu)) break;
      }
      cfence();
      float qs[4];
#pragma unroll
      for (int r = 0; r < 4; r++) qs[r] = (__uint_as_float(hi2(qv[1][r], qv[0][r])) + __uint_as_float(hi2(qv[3][r], qv[2][r]))) + __uint_as_float(hi2(qv[5][r], qv[4][r]));
      *(LAS u32x2*)(L + L_Q + lane * 8) = u32x2{pk2(qs[0], qs[1]), pk2(qs[2], qs[3])};
      const int dt = (int)(tc - (tstart << 16)) >> 16;
      const bool tv = ((tc ^ E) & 0xffffu) == 0 && dt < 600 && dt > -600;
      float dm = tv ? (float)dt : -600.f, dn = tv ? (float)dt : 600.f;
      dm = fmaxf(dm, dppf<XOR1>(dm)); dm = fmaxf(dm, dppf<XOR2>(dm)); dm = fmaxf(dm, dppf<HMIR>(dm));
      dn = fminf(dn, dppf<XOR1>(dn)); dn = fminf(dn, dppf<XOR2>(dn)); dn = fminf(dn, dppf<HMIR>(dn));
      dm -= fminf(fmaxf(dm - dn - 60.f, 0.f), (float)GREC);
      if (lane == 0) { *(LAS unsigned*)(L + L_CNT + 60) = E; *(LAS unsigned*)(L + L_CNT + 56) = tstart + (int)dm - GL; }
      lds_arrive(L, 8);
    }
    STAMP(4);
    lds_arrive(L, 2);
    lds_wait<8>(L, 5, 4); lds_wait<2>(L, 9, 4);
    if (proj) {
#pragma unroll
      for (int i = 0; i < 24; i++) *(LAS u32x4*)(L + L_WO + sw * 3072 + lane * 16 + (i / 3) * 12288 + (i % 3) * 1024) = ew[i];
      lds_arrive(L, 10);
    }
    if (mlp) {
#pragma unroll
      for (int i = 0; i < 18; i++) *(LAS u32x4*)(L + L_W1 + sw * 1024 + lane * 16 + i * 4096) = ew[i];
      lds_arrive(L, 10);
      {
        const int l48 = lane < 48 ? lane : 47, bb = grp * 4 + sw;
        u32x4 sbv[3], cbv[3], gav, gbv;
        _Pragma("unroll 1") for (unsigned s = 0; s < SPIN_CAP; s++) {
          cfence(); unsigned bad = 0;
#pragma unroll
          for (int i = 0; i < 3; i++) { sbv[i] = ld128(rs, WS_MODR + (bb * 2304 + 576 + 192 * i + 4 * l48) * 4); cbv[i] = ld128(rs, WS_MODR + (bb * 2304 + 1152 + 192 * i + 4 * l48) * 4); }
          gav = ld128(rs, WS_MODR + (tb * 2304 + tcol) * 4); gbv = ld128(rs, WS_MODR + (tb * 2304 + 1728 + tcol) * 4);
#pragma unroll
          for (int i = 0; i < 3; i++) bad |= xor4(sbv[i], E) | xor4(cbv[i], E);
          if (all_ok((bad | xor4(gav, E) | xor4(gbv, E)) & 0xffffu)) break;
        }
        cfence();
#pragma unroll
        for (int i = 0; i < 6; i++) { g1p[i] = pk2(1.f + hi2f(cbv[i >> 1][(2 * i) & 3]), 1.f + hi2f(cbv[i >> 1][(2 * i + 1) & 3])); sbp[i] = hi2(sbv[i >> 1][(2 * i) & 3], sbv[i >> 1][(2 * i + 1) & 3]); pin(g1p[i]); pin(sbp[i]); }
        if (sw == 3 && lane < 8) { const u32x2 xr = *(const u32x2*)(x + (tb * 576 + tcol) * 2);
          *(LAS u32x4*)(L + L_TP + lane * 48) = gav & 0xffff0000u; *(LAS u32x4*)(L + L_TP + lane * 48 + 16) = gbv & 0xffff0000u;
          *(LAS u32x4*)(L + L_TP + lane * 48 + 32) = u32x4{xr[0] << 16, xr[0] & 0xffff0000u, xr[1] << 16, xr[1] & 0xffff0000u}; }
      }
      lds_arrive(L, 9);
    }
    const char* w2b = w2 + (size_t)slice * 73728;
    if (mlp && sw > 0) { _Pragma("unroll 1") for (int gi = sw - 1; gi < NE2; gi += 3) { dma1<WNT>(w2b + gi * 1024, lane * 16, L0 + L_W2 + gi * 1024); waitvm<W1W>(); } }
    STAMP(5);
    if (sw == 0) {
      lds_wait<8>(L, 2, 8);
      const int hh = lane & 1, ww = (lane >> 1) & 3;
      float mw[4], lw[4];
#pragma unroll
      for (int i = 0; i < 4; i++) { const f32x2 v = *(LAS f32x2*)(L + L_ML + i * 16 + hh * 8); mw[i] = v[0]; lw[i] = v[1]; }
      const float Mc = fmaxf(fmaxf(mw[0], mw[1]), fmaxf(mw[2], mw[3])), Ms = (Mc == -INFINITY) ? 0.f : Mc;
      float Lc = 0;
#pragma unroll
      for (int i = 0; i < 4; i++) Lc += lw[i] * __builtin_amdgcn_exp2f((mw[i] - Ms) * 1.4426950408889634f);
      const unsigned tm = tag16(Mc, E), tl = tag10(Lc, E & 0x3ffu);
      const u32x4 rec = {dppu<0x00>(tm), dppu<0x55>(tm), dppu<0x00>(tl), dppu<0x55>(tl)};
      if (lane == 0) st128(rs, WS_ML + bid * 128, rec);
      u32x4 o;
      _Pragma("unroll 1") for (unsigned s = 0; s < SPIN_CAP; s++) {
        cfence();
        o = ld128(rs, WS_ML + (bid ^ 128) * 128);
        if (all_ok((((o[0] ^ E) | (o[1] ^ E)) & 0xffffu) | (((o[2] ^ E) | (o[3] ^ E)) & 0x3ffu))) break;
        __builtin_amdgcn_s_sleep(8);
      }
      cfence();
      const float Mo = hi2f(hh ? o[1] : o[0]), Lo = un10(hh ? o[3] : o[2]);
      const float M = fmaxf(Mc, Mo);
      const float Lt = un10(tl) * __builtin_amdgcn_exp2f((Mc - M) * 1.4426950408889634f) + Lo * __builtin_amdgcn_exp2f((Mo - M) * 1.4426950408889634f);
      const float mine = *(LAS float*)(L + L_ML + ww * 16 + hh * 8);
      if (lane < 8) *(LAS float*)(L + L_SC + lane * 4) = __builtin_amdgcn_exp2f((mine - M) * 1.4426950408889634f) / Lt;
      lds_arrive(L, 3);
    }
    STAMP(14);
    if ((WARM & 2) && idx == 31 && sw == 1) { until(tstart + TWARM); warm(); }
    if (mlp) {
      lds_wait<1>(L, 5, 8);
      _Pragma("unroll 1") for (int gi = NE2 + sw; gi < 72; gi += 4) { dma1<WNT>(w2b + gi * 1024, lane * 16, L0 + L_W2 + gi * 1024); waitvm<WL2>(); }
      waitvm<0>();
      lds_arrive(L, 10);
    }
    if (PRIO && mlp) __builtin_amdgcn_s_setprio(0);
    STAMP(6);
  }
  const int n4 = n >> 2;
  const unsigned tend = __builtin_amdgcn_readfirstlane(*(volatile LAS unsigned*)(L + L_CNT + 56)) + GOFF + 63 * GTAU / 10 + TEND;
  unsigned E10 = E & 0x3ffu;
  if (proj) {
    const int pw = w ^ 4;
    if (pw < 6) {
      lds_wait<1>(L, 10, 4);
      LAS char* p = L + L_WO + (8 * g + (n >> 2)) * 384 + (n & 3) * 8 + pw * 64;
#pragma unroll
      for (int s = 0; s < 8; s++)
#pragma unroll
        for (int u = 0; u < 2; u++) { const u32x2 a = trread(p + s * 12288 + u * 32), c2 = trread(p + s * 12288 + u * 32 + 1536); wof[u][s] = u32x4{a[0], a[1], c2[0], c2[1]}; pin(wof[u][s]); }
    }
    int p1off = WS_P1 + (((n & 7) * 16 + pk) * 576 + cs * 192 + pw * 32 + (n >> 3) * 16 + 4 * g) * 4;
    unsigned ob = L_OB + (n & 7) * OB_STR + g * 16;
    pin(p1off); pin(ob); pin(E10);
    if (w >= 4) {
      int go = WS_OG + (pk * 8 + 2 * (w - 4)) * 2048 + lane * 16; unsigned ow = L_OB + 2 * (w - 4) * OB_STR + lane * 8;
      pin(go); pin(ow);
      until(tend + FO); lds_wait<0>(L, 1, 4); __builtin_amdgcn_s_sleep(SO);
      STAMP(8);
      u32x4 p[4];
      _Pragma("unroll 1") for (unsigned s = 0; s < SPIN_CAP; s++) {
        cfence(); unsigned bad = 0;
#pragma unroll
        for (int i = 0; i < 4; i++) p[i] = __builtin_amdgcn_raw_buffer_load_b128(rs, go, i * 1024, 16);
#pragma unroll
        for (int i = 0; i < 4; i++) bad |= xor4(p[i], E);
        if (all_ok(bad & 0x3ffu)) break;
      }
      cfence();
      STAMP(9);
#pragma unroll
      for (int i = 0; i < 2; i++) *(LAS u32x2*)(L + ow + i * OB_STR) = u32x2{pk2(un10(p[2 * i][0]) + un10(p[2 * i + 1][0]), un10(p[2 * i][1]) + un10(p[2 * i + 1][1])),
                                                                             pk2(un10(p[2 * i][2]) + un10(p[2 * i + 1][2]), un10(p[2 * i][3]) + un10(p[2 * i + 1][3]))};
    }
    wg_barrier();
    STAMP(10);
    if (pw < 6) {
      f32x4 d0 = {0, 0, 0, 0}, d1 = {0, 0, 0, 0}; bf16x8 of[8];
#pragma unroll
      for (int s = 0; s < 8; s++) of[s] = *(LAS bf16x8*)(L + ob + s * 64);
#pragma unroll
      for (int s = 0; s < 8; s++) { d0 = mfma32(asbf8(wof[0][s]), of[s], d0); d1 = mfma32(asbf8(wof[1][s]), of[s], d1); }
      u32x4 y;
#pragma unroll
      for (int r = 0; r < 4; r++) y[r] = tag10(n < 8 ? d0[r] : d1[r], E10);
      st128(rs, p1off, y);
    }
    cfence();
    STAMP(11);
  }
  if (mlp) {
    u32x4 w2a[5], w2b[5];
    auto w2read = [&]() {
      LAS char* pa = L + L_W2 + (8 * g + (n >> 2)) * 1152 + (n & 3) * 8;
      LAS char* pb = L + L_W2 + (32 + 8 * g + (n >> 2)) * 1152 + (n & 3) * 8;
#pragma unroll
      for (int u = 0; u < 5; u++) { const int o = u < 4 ? w * 128 + u * 32 : 1024 + (w & 3) * 32;
        const u32x2 a = trread(pa + o), c2 = trread(pa + o + 4608); w2a[u] = u32x4{a[0], a[1], c2[0], c2[1]};
        const u32x2 a2 = trread(pb + o), c3 = trread(pb + o + 4608); w2b[u] = u32x4{a2[0], a2[1], c3[0], c3[1]}; } };
    f32x4 x1v, gbv;
    auto w2get = [&]() { lds_wait<1>(L, 10, 8); w2read();
#pragma unroll
      for (int u = 0; u < 5; u++) { pin(w2a[u]); pin(w2b[u]); } };
    if (w != 3) w2get();
    if (w == 3) {
      lds_wait<1>(L, 9, 8); lds_wait<1>(L, 1, 4);
      {
        const f32x4 gav = *(LAS f32x4*)(L + L_TP + (lane & 7) * 48), xv = *(LAS f32x4*)(L + L_TP + (lane & 7) * 48 + 32);
        gbv = *(LAS f32x4*)(L + L_TP + (lane & 7) * 48 + 16);
        int po = WS_P1 + ((tb * 16 + p8) * 576 + tcol) * 4, xo = WS_X1 + p8 * X1_REP + (tb * 576 + tcol) * 4;
        pin(po); pin(xo);
        STAMP(5);
        __builtin_amdgcn_s_sleep(SB); until(tend + FB);
        u32x4 p[2];
        _Pragma("unroll 1") for (unsigned s = 0; s < SPIN_CAP; s++) {
          cfence();
          p[0] = ld128(rs, po); p[1] = __builtin_amdgcn_raw_buffer_load_b128(rs, po, 8 * 2304, 16);
          if (all_ok((xor4(p[0], E) | xor4(p[1], E)) & 0x3ffu)) break;
          }
        lds_arrive(L, 11);
        STAMP(6);
        u32x4 y;
#pragma unroll
        for (int r = 0; r < 4; r++) {
          float s = un10(p[0][r]) + un10(p[1][r]);
          s += dppf<ROR8>(s); s = x16sum(s); s = x32sum(s);
          y[r] = tag16(xv[r] + rbf(gav[r] * rbf(s)), E); x1v[r] = hi2f(y[r]);
        }
        st128(rs, xo, y);
        cfence();
        STAMP(9);
      }
      w2get();
    } else if (w >= 4) {
      int xo = WS_X1 + xcd * X1_REP + ((grp * 4 + w - 4) * 576 + 4 * (lane < 48 ? lane : 47)) * 4;
      unsigned ha = L_H2 + (w - 4) * H2_STR + lane * 8;
      float g1[12], sb[12];
#pragma unroll
      for (int i = 0; i < 12; i++) { g1[i] = (i & 1) ? hi2f(g1p[i >> 1]) : bf2f(g1p[i >> 1]); sb[i] = (i & 1) ? hi2f(sbp[i >> 1]) : bf2f(sbp[i >> 1]); pin(g1[i]); pin(sb[i]); }
      pin(xo); pin(ha);
      until(tend + FX); lds_wait<0>(L, 11, 1); __builtin_amdgcn_s_sleep(SX);
      STAMP(9);
      u32x4 xv[3];
      _Pragma("unroll 1") for (unsigned s = 0; s < SPIN_CAP; s++) {
        cfence(); unsigned bad = 0;
#pragma unroll
        for (int i = 0; i < 3; i++) xv[i] = ld128(rs, xo + 768 * i);
#pragma unroll
        for (int i = 0; i < 3; i++) bad |= xor4(xv[i], E);
        if (all_ok(bad & 0xffffu)) break;
      }
      cfence();
      STAMP(10);
      const float x0 = hi2f(__builtin_amdgcn_readfirstlane(xv[0][0]));
      float xf[12], s1 = 0, s2 = 0;
#pragma unroll
      for (int i = 0; i < 12; i++) { xf[i] = hi2f(xv[i >> 2][i & 3]); const float dd = xf[i] - x0; s1 += dd; s2 += dd * dd; }
      if (lane >= 48) { s1 = 0; s2 = 0; }
      s1 += dppf<XOR1>(s1); s2 += dppf<XOR1>(s2); s1 += dppf<XOR2>(s1); s2 += dppf<XOR2>(s2);
      s1 += dppf<ROR4>(s1); s2 += dppf<ROR4>(s2); s1 += dppf<ROR8>(s1); s2 += dppf<ROR8>(s2);
      s1 = x16sum(s1); s2 = x16sum(s2); s1 = x32sum(s1); s2 = x32sum(s2);
      const float md = s1 * (1.f / 576.f), var = fmaxf(s2 * (1.f / 576.f) - md * md, 0.f);
      const float mb = rbf(x0 + md), rs_ = __builtin_amdgcn_rcpf(rbf(__builtin_amdgcn_sqrtf(rbf(rbf(var) + 1e-5f))));
      if (lane < 48) {
#pragma unroll
        for (int i = 0; i < 3; i++) { float hh[4];
#pragma unroll
          for (int j = 0; j < 4; j++) hh[j] = (xf[4 * i + j] - mb) * rs_ * g1[4 * i + j] + sb[4 * i + j];
          *(LAS u32x2*)(L + ha + 384 * i) = u32x2{pk2(hh[0], hh[1]), pk2(hh[2], hh[3])}; }
      }
    }
    int po = WS_P2 + (((grp * 4 + (n & 3)) * 72 + slice) * 576 + 4 * g) * 4, po2 = n < 4 ? po + (448 + 16 * w) * 4 : WS_END;
    po += (64 * w + 16 * n4) * 4;
    int fo = WS_P2 + ((tb * 72 + p8) * 576 + tcol) * 4;
    unsigned h1a = L_H1 + (n & 3) * H1_STR + g * 16, h2a = L_H2 + (n & 3) * H2_STR + g * 16, h1w = L_H1 + n * H1_STR + (w * 16 + 4 * g) * 2;
    pin(po); pin(po2); pin(h1a); pin(h2a); pin(h1w); pin(fo);
    wg_barrier();
    STAMP(11);
    if (w < 4) {
      f32x4 d = {0, 0, 0, 0}; bf16x8 hf[18];
#pragma unroll
      for (int s = 0; s < 18; s++) hf[s] = *(LAS bf16x8*)(L + h2a + s * 64);
#pragma unroll
      for (int s = 0; s < 18; s++) d = mfma32(asbf8(w1f[s]), hf[s], d);
#pragma unroll
      for (int r = 0; r < 4; r++) d[r] = d[r] < 0.f ? 0.f : d[r];
      if (n < 4) *(LAS u32x2*)(L + h1w) = u32x2{pk2(d[0], d[1]), pk2(d[2], d[3])};
    }
    wg_barrier();
    STAMP(12);
    {
      const bf16x8 ha = *(LAS bf16x8*)(L + h1a), hb = *(LAS bf16x8*)(L + h1a + 64);
      f32x4 d[5];
#pragma unroll
      for (int u = 0; u < 4; u++) d[u] = mfma32(asbf8(w2b[u]), hb, mfma32(asbf8(w2a[u]), ha, f32x4{0, 0, 0, 0}));
      if (w >= 4) d[4] = mfma32(asbf8(w2b[4]), hb, mfma32(asbf8(w2a[4]), ha, f32x4{0, 0, 0, 0}));
      u32x4 y;
#pragma unroll
      for (int r = 0; r < 4; r++) y[r] = tag10(sel4(d[0][r], d[1][r], d[2][r], d[3][r]), E10);
      st128(rs, po, y);
      if (w >= 4) st128(rs, po2, u32x4{tag10(d[4][0], E10), tag10(d[4][1], E10), tag10(d[4][2], E10), tag10(d[4][3], E10)});
    }
    cfence();
    if (w == 3) {
      STAMP(13);
      __builtin_amdgcn_s_sleep(SC); if (FC) until(tend + FC);
      u32x4 p[9];
      _Pragma("unroll 1") for (unsigned s = 0; s < SPIN_CAP; s++) {
        cfence(); unsigned bad = 0;
#pragma unroll
        for (int i = 0; i < 9; i++) p[i] = __builtin_amdgcn_raw_buffer_load_b128(rs, fo, i * 8 * 2304, 16);
#pragma unroll
        for (int i = 0; i < 9; i++) bad |= xor4(p[i], E);
        if (all_ok(bad & 0x3ffu)) break;
      }
      cfence();
      STAMP(14);
      float o[4];
#pragma unroll
      for (int r = 0; r < 4; r++) { float t = 0;
#pragma unroll
        for (int i = 0; i < 3; i++) t += (un10(p[3 * i][r]) + un10(p[3 * i + 1][r])) + un10(p[3 * i + 2][r]);
        t += dppf<ROR8>(t); t = x16sum(t); t = x32sum(t); o[r] = x1v[r] + rbf(gbv[r] * rbf(t)); }
      if (lane < 8) *(u32x2*)(out + (tb * 576 + tcol) * 2) = u32x2{pk2(o[0], o[1]), pk2(o[2], o[3])};
    }
  } else if (bid == 248 && w == 1) {
    _Pragma("unroll 1") for (unsigned s = 0; s < SPIN_CAP; s++) {
      cfence();
      const u32x4 v = ld128(rs, WS_X1 + (lane & 7) * 576 * 4);
      if (all_ok(xor4(v, E) & 0xffffu)) break;
      __builtin_amdgcn_s_sleep(4);
    }
    cfence();
    if (lane == 0) st32(rs, WS_EPOCH, E);
  }
  STAMP(15);
  if (w == 7) { const unsigned tend = pt0 + 9000 + (1) * (int)(ptp - pt0); _Pragma("unroll 1") while ((int)((unsigned)__builtin_amdgcn_s_memrealtime() - tend) < 0) __builtin_amdgcn_s_sleep(1); }
  asm volatile(".Lfb_end:");
}

extern "C" void kernel_launch(void* const* d_in, const int* in_sizes, int n_in, void* d_out, int out_size, void* d_ws, size_t ws_size, hipStream_t stream) {
  fused_block<<<256, 512, 0, stream>>>((const char*)d_in[1], (const char*)d_in[2], (const char*)d_in[0], (const char*)d_in[3], (const char*)d_in[4], (const char*)d_in[6],
                                       (const char*)d_in[7], (const char*)d_in[8], (const char*)d_in[9], (char*)d_out, (char*)d_ws);
}
```

```cpp
#include <hip/hip_runtime.h>
#include <hip/hip_bf16.h>
#include <stdint.h>
typedef unsigned short u16;
typedef unsigned long long u64;
typedef short s16x4 __attribute__((ext_vector_type(4)));
typedef __bf16 bf16x8 __attribute__((ext_vector_type(8)));
typedef __bf16 bf16x2 __attribute__((ext_vector_type(2)));
typedef float f32x4 __attribute__((ext_vector_type(4)));
typedef float f32x2 __attribute__((ext_vector_type(2)));
typedef unsigned u32x4 __attribute__((ext_vector_type(4)));
typedef unsigned u32x2 __attribute__((ext_vector_type(2)));
#define LAS __attribute__((address_space(3)))
#define DEVI __device__ __forceinline__

constexpr int WS_EPOCH = 0;
constexpr int WS_FT = 1024;
constexpr int WS_HG = 4096, HG_REP = 18 * 2048;
constexpr int WS_QG = WS_HG + 8 * HG_REP, QG_PL = 8 * 4096 * 4;
constexpr int WS_MODR = WS_QG + 6 * QG_PL;
constexpr int WS_ML = WS_MODR + 8 * 2304 * 4;
constexpr int WS_OG = WS_ML + 256 * 128;
constexpr int WS_P1 = WS_OG + 256 * 1024;
constexpr int WS_X1 = WS_P1 + 8 * 16 * 576 * 4, X1_REP = 8 * 576 * 4;
constexpr int WS_P2 = WS_X1 + 8 * X1_REP;
constexpr int WS_END = WS_P2 + 8 * 72 * 576 * 4;
constexpr int L_W2 = 49152, L_W1 = L_W2 + 36864;
constexpr int L_RED = 131072, L_NX = L_RED + 17408;
constexpr int L_WO = 49152, L_OB = L_WO + 98304, OB_STR = 528;
constexpr int L_MISC = 159744;
constexpr int L_CNT = L_MISC, L_ML = L_MISC + 64, L_SC = L_MISC + 128, L_Q = L_MISC + 256, L_H1 = L_MISC + 1024, L_TP = L_MISC + 2048;
constexpr int H2_STR = 1216, H1_STR = 144;
constexpr int L_TOTAL = 163840;
constexpr unsigned SPIN_CAP = 400000;

DEVI float bf2f(unsigned v) { return __uint_as_float(v << 16); }
DEVI float hi2f(unsigned v) { return __uint_as_float(v & 0xffff0000u); }
DEVI unsigned pk2(float lo, float hi) { bf16x2 v = {(__bf16)lo, (__bf16)hi}; return __builtin_bit_cast(unsigned, v); }
DEVI float rbf(float f) { return hi2f(pk2(f, f)); }
template <int CTRL> DEVI unsigned dppu(unsigned x) { return (unsigned)__builtin_amdgcn_mov_dpp((int)x, CTRL, 0xf, 0xf, true); }
template <int CTRL> DEVI float dppf(float x) { return __uint_as_float(dppu<CTRL>(__float_as_uint(x))); }
constexpr int XOR1 = 0xB1, XOR2 = 0x4E, HMIR = 0x141, ROR4 = 0x124, ROR8 = 0x128;
DEVI float x16sum(float x) { auto s = __builtin_amdgcn_permlane16_swap(__float_as_uint(x), __float_as_uint(x), false, false); return __uint_as_float(s[0]) + __uint_as_float(s[1]); }
DEVI float x32sum(float x) { auto s = __builtin_amdgcn_permlane32_swap(__float_as_uint(x), __float_as_uint(x), false, false); return __uint_as_float(s[0]) + __uint_as_float(s[1]); }
DEVI float x16max(float x) { auto s = __builtin_amdgcn_permlane16_swap(__float_as_uint(x), __float_as_uint(x), false, false); return fmaxf(__uint_as_float(s[0]), __uint_as_float(s[1])); }
DEVI float x32max(float x) { auto s = __builtin_amdgcn_permlane32_swap(__float_as_uint(x), __float_as_uint(x), false, false); return fmaxf(__uint_as_float(s[0]), __uint_as_float(s[1])); }
DEVI unsigned lo2(unsigned a, unsigned b) { return __builtin_amdgcn_perm(b, a, 0x05040100u); }
DEVI unsigned hi2(unsigned a, unsigned b) { return __builtin_amdgcn_perm(b, a, 0x07060302u); }
DEVI bf16x8 asbf8(u32x4 v) { return __builtin_bit_cast(bf16x8, v); }
DEVI f32x4 mfma32(bf16x8 a, bf16x8 b, f32x4 c) { return __builtin_amdgcn_mfma_f32_16x16x32_bf16(a, b, c, 0, 0, 0); }
DEVI f32x4 mfma16(u32x2 a, u32x2 b, f32x4 c) { return __builtin_amdgcn_mfma_f32_16x16x16bf16_1k(__builtin_bit_cast(s16x4, a), __builtin_bit_cast(s16x4, b), c, 0, 0, 0); }
DEVI u32x2 trread(LAS char* p) { return __builtin_bit_cast(u32x2, __builtin_amdgcn_ds_read_tr16_b64_v4i16((LAS s16x4*)p)); }
DEVI void cfence() { asm volatile("" ::: "memory"); }
DEVI void wg_barrier() { asm volatile("s_waitcnt lgkmcnt(0)\n\ts_barrier" ::: "memory"); }
template <int N> DEVI void waitvm() { asm volatile("s_waitcnt vmcnt(%0)" ::"i"(N) : "memory"); }
DEVI void waitlgkm() { asm volatile("s_waitcnt lgkmcnt(0)" ::: "memory"); }
typedef __amdgpu_buffer_rsrc_t rsrc_t;
DEVI u32x4 ld128(rsrc_t rs, int off) { return __builtin_amdgcn_raw_buffer_load_b128(rs, off, 0, 16); }
DEVI void st128(rsrc_t rs, int off, u32x4 v) { __builtin_amdgcn_raw_buffer_store_b128(v, rs, off, 0, 16); }
DEVI rsrc_t mkrs(const char* p, int bytes) { return __builtin_amdgcn_make_buffer_rsrc((void*)p, 0, bytes, 0x00020000); }
template <int BANK> DEVI float banksel(float old, float v) { return __uint_as_float((unsigned)__builtin_amdgcn_update_dpp((int)__float_as_uint(old), (int)__float_as_uint(v), 0xE4, 0xf, 1 << BANK, false)); }
DEVI float sel4(float a, float b, float c, float d) { return banksel<3>(banksel<2>(banksel<1>(a, b), c), d); }
template <class T> DEVI void pin(T& v) { asm volatile("" : "+v"(v)); }
DEVI unsigned ld32(rsrc_t rs, int off) { return __builtin_amdgcn_raw_buffer_load_b32(rs, off, 0, 16); }
DEVI void st32(rsrc_t rs, int off, unsigned v) { __builtin_amdgcn_raw_buffer_store_b32(v, rs, off, 0, 16); }
DEVI unsigned xor4(u32x4 v, unsigned t) { return (v[0] ^ t) | (v[1] ^ t) | (v[2] ^ t) | (v[3] ^ t); }
DEVI bool all_ok(unsigned bad) { return __builtin_amdgcn_ballot_w64(bad != 0) == 0; }
DEVI unsigned tag16(float v, unsigned t) { return (pk2(v, v) & 0xffff0000u) | (t & 0xffffu); }
DEVI unsigned tag10(float v, unsigned t10) { return (__float_as_uint(v) & ~0x3ffu) | t10; }
DEVI float un10(unsigned v) { return __uint_as_float(v & ~0x3ffu); }
DEVI unsigned now() { return (unsigned)__builtin_amdgcn_s_memrealtime(); }
DEVI void until(unsigned t) {
  _Pragma("unroll 1") while ((int)(now() - t) < -50) __builtin_amdgcn_s_sleep(10);
  _Pragma("unroll 1") while ((int)(now() - t) < 0) {}
  cfence(); }
DEVI void lds_arrive(LAS char* L, int i) { waitlgkm(); if ((threadIdx.x & 63) == 0) __hip_atomic_fetch_add((LAS unsigned*)(L + L_CNT + 4 * i), 1u, __ATOMIC_RELAXED, __HIP_MEMORY_SCOPE_WORKGROUP); cfence(); }
template <int SLEEP> DEVI void lds_wait(LAS char* L, int i, unsigned target) {
  _Pragma("unroll 1") for (unsigned s = 0; s < 16 * SPIN_CAP; s++) {
    unsigned v = *(volatile LAS unsigned*)(L + L_CNT + 4 * i);
    if (__builtin_amdgcn_readfirstlane(v) >= target) break;
    if (SLEEP) __builtin_amdgcn_s_sleep(SLEEP);
  }
  cfence();
}
#define DMA4_BODY(POL) asm volatile("s_mov_b32 %0, m0\n\ts_mov_b32 m0, %6\n\ts_nop 0\n\t" \
     "global_load_lds_dwordx4 %1, %5 offset:0" POL "\n\tglobal_load_lds_dwordx4 %2, %5 offset:1024" POL "\n\t" \
     "global_load_lds_dwordx4 %3, %5 offset:2048" POL "\n\tglobal_load_lds_dwordx4 %4, %5 offset:3072" POL "\n\ts_mov_b32 m0, %0" \
     : "=&s"(keep) : "v"(o[0]), "v"(o[1]), "v"(o[2]), "v"(o[3]), "s"(sbase), "s"(dst) : "memory")
template <int NT> DEVI void dma4(const char* sbase, const unsigned (&o)[4], unsigned dst) { unsigned keep; if (NT) DMA4_BODY(" nt"); else DMA4_BODY(""); }
template <int NT> DEVI void dma1(const char* sbase, unsigned o, unsigned dst) {
  unsigned keep;
  if (NT) asm volatile("s_mov_b32 %0, m0\n\ts_mov_b32 m0, %3\n\ts_nop 0\n\tglobal_load_lds_dwordx4 %1, %2 nt\n\ts_mov_b32 m0, %0" : "=&s"(keep) : "v"(o), "s"(sbase), "s"(dst) : "memory");
  else asm volatile("s_mov_b32 %0, m0\n\ts_mov_b32 m0, %3\n\ts_nop 0\n\tglobal_load_lds_dwordx4 %1, %2\n\ts_mov_b32 m0, %0" : "=&s"(keep) : "v"(o), "s"(sbase), "s"(dst) : "memory");
}
#define WNT 0
template <int R> DEVI unsigned ror(unsigned x) { if constexpr (R == 0) return x; else return dppu<0x120 + R>(x); }
template <int T8, int U> DEVI void pv1(f32x4& acc, unsigned z0, unsigned z1, u32x2 v) {
  constexpr int R = (2 * (U - T8) + 16) & 15;
  acc = mfma16(v, u32x2{ror<R>(z0), ror<R>(z1)}, acc);
}
#define NPRE 12
constexpr int NF = 8 + NPRE;
constexpr int slot_of(int t) { return t < NF ? (t + 16 - NPRE) % 8 : (t - NF) % 3; }
template <int WT> DEVI void dma4_paced(const char* sbase, const unsigned (&o)[4], unsigned dst) { unsigned keep;
  asm volatile("s_mov_b32 %0, m0\n\ts_mov_b32 m0, %6\n\ts_waitcnt vmcnt(%7) lgkmcnt(0)\n\t"
     "global_load_lds_dwordx4 %1, %5 offset:0 nt\n\ts_waitcnt vmcnt(%7)\n\tglobal_load_lds_dwordx4 %2, %5 offset:1024 nt\n\ts_waitcnt vmcnt(%7)\n\t"
     "global_load_lds_dwordx4 %3, %5 offset:2048 nt\n\ts_waitcnt vmcnt(%7)\n\tglobal_load_lds_dwordx4 %4, %5 offset:3072 nt\n\ts_mov_b32 m0, %0"
     : "=&s"(keep) : "v"(o[0]), "v"(o[1]), "v"(o[2]), "v"(o[3]), "s"(sbase), "s"(dst), "i"(WT) : "memory"); }
#define W1W 1
#define DEEP 4
#define PRIO 2
#define NE2 0
#define TLATE 60
#define FO -25
#define SO 8
#define FB 145
#define SB 36
#define FX 0
#define SX 13
#define SC 25
#define FC 0
#define WARM 3
#define TWARM 3700
#define GREC 150
#define GST 15
#define GL 30
#define TEND 115
#define TW1 34
#define WL2 6
#define TH 480
#define TQ 850
#define GOFF 160
#define GTAU 618
#define PW 6
constexpr int OSLOT = slot_of(62), L_H2 = slot_of(63) * 16384;
static_assert(NPRE >= 6 && NPRE <= 12 && NE2 <= 36 && TW1 <= TLATE, "schedule");
template <int I, int N, class F> DEVI void sfor(F&& f) { if constexpr (I < N) { f(std::integral_constant<int, I>{}); sfor<I + 1, N>(f); } }

extern "C" __global__ void __launch_bounds__(512, 2) fused_block(const char* Kc, const char* Vc, const char* x, const char* wq, const char* wo, const char* w1,
                                                      const char* w2, const char* cc, const char* wm, char* out, char* ws) {
  __shared__ __attribute__((aligned(16))) char lds_mem[L_TOTAL];
  LAS char* L = (LAS char*)lds_mem;
  const unsigned L0 = (unsigned)(size_t)L;
  const int lane = threadIdx.x & 63, w = __builtin_amdgcn_readfirstlane(threadIdx.x >> 6);
  const int bid = blockIdx.x, xcd = bid & 7, idx = bid >> 3;
  const int kvh = 2 * xcd + (idx & 1), b = (idx >> 1) & 7, half = idx >> 4;
  const int n = lane & 15, g = lane >> 4;
  const rsrc_t rs = __builtin_amdgcn_make_buffer_rsrc((void*)ws, 0, WS_END, 0x00020000);
  const unsigned tstart = now();
  if (threadIdx.x < 12) *(LAS unsigned*)(L + L_CNT + threadIdx.x * 4) = 0;
  wg_barrier();
  u32x4 wof[2][8];
  unsigned E;
  unsigned g1p[6], sbp[6];
  const int i12 = idx % 12, grp = idx / 12, slice = 9 * xcd + i12;
  const bool mlp = idx < 24 && i12 < 9, proj = idx < 24 && i12 >= 9;
  const int tb = 4 * grp + (xcd >> 1), tcol = 32 * (9 * (xcd & 1) + i12) + 4 * (lane & 7), p8 = lane >> 3;
  const int j1 = (idx - 24) * 8 + xcd;
  const bool s1 = idx >= 24 && j1 < 54;
  const int pk = 2 * xcd + grp, cs = i12 - 9;
  u32x4 w1f[18];

  if (w < 4) {
    const size_t base = (size_t)(b * 16 + kvh) * 1048576 + (size_t)half * 524288 + w * 4096;
    const char* kb = Kc + base; const char* vb = Vc + base;
    unsigned tg0 = tstart + GOFF + w * GST;
    auto gate = [&](int t) {
      if (GTAU) { _Pragma("unroll 1") while ((int)((unsigned)__builtin_amdgcn_s_memrealtime() - tg0 - t * GTAU / 10) < 0) {} } };
    unsigned ko[4], vo[4];
#pragma unroll
    for (int i = 0; i < 4; i++) { const int row = 4 * i + g; ko[i] = g * 256 + ((n ^ row) * 16); vo[i] = g * 256 + ((n ^ ((row & 7) << 1)) * 16); }
    const unsigned ring0 = L0 + w * 4096;
    LAS char* ring = L + w * 4096;
    unsigned kr[4];
#pragma unroll
    for (int s = 0; s < 4; s++) kr[s] = n * 256 + (((4 * s + g) ^ n) * 16);
    auto issue = [&](auto T) { constexpr int t = T; if constexpr (t < 64) gate(t); waitlgkm();
      if constexpr (t < 32) dma4<1>(kb + t * 16384, ko, ring0 + slot_of(t) * 16384);
      else if constexpr (t < 64) dma4<1>(vb + (t - 32) * 16384, vo, ring0 + slot_of(t) * 16384); };
    bf16x8 pre[NPRE][4];
    lds_wait<0>(L, 4, 4);
    sfor<0, NF>([&](auto T) { constexpr int t = T;
      if constexpr (PW > 0) { gate(t); dma4_paced<PW>(kb + t * 16384, ko, ring0 + slot_of(t) * 16384); }
      else { if constexpr (t >= 3) waitvm<8>(); issue(T); }
      if constexpr (t >= 3 && t - 3 < NPRE) {
#pragma unroll
        for (int s = 0; s < 4; s++) pre[t - 3][s] = *(LAS bf16x8*)(ring + slot_of(t - 3) * 16384 + kr[s]); } });
    const int vrow = 4 * g + (n >> 2), vp = n & 3, n2 = n >> 1;
    const unsigned vr0 = vrow * 256 + (vp >> 1) * 16 + (vp & 1) * 8, vx = (vrow & 7) << 5;
    lds_wait<0>(L, 8, 1);
    bf16x8 qf[4];
    f32x4 S[4] = {{0, 0, 0, 0}, {0, 0, 0, 0}, {0, 0, 0, 0}, {0, 0, 0, 0}};
    auto score = [&](auto T, const bf16x8 (&kf)[4]) { constexpr int t = T;
      f32x4 d = {0, 0, 0, 0};
#pragma unroll
      for (int s = 0; s < 4; s++) d = mfma32(kf[s], qf[s], d);
#pragma unroll
      for (int r = 0; r < 4; r++) S[t >> 3][r] = (n2 == (t & 7)) ? d[r] : S[t >> 3][r]; };
    auto kread = [&](auto T, bf16x8 (&kf)[4]) { constexpr int t = T;
#pragma unroll
      for (int s = 0; s < 4; s++) kf[s] = *(LAS bf16x8*)(ring + slot_of(t) * 16384 + kr[s]); };
    auto wwait = [&](auto T) { constexpr int t = T; waitvm<(t + 1 < 64 ? 4 : 0) + (t + 2 < 64 ? 4 : 0)>(); };
    auto wnext = [&](auto T) { constexpr int t = T;
      if constexpr (DEEP < 4 && t + 3 < 64) waitvm<4 + DEEP>();
      issue(std::integral_constant<int, t + 3>{}); };
#pragma unroll
    for (int s = 0; s < 4; s++) qf[s] = *(LAS bf16x8*)(L + L_Q + (n & 1) * 256 + (4 * s + g) * 16);
    E = *(LAS unsigned*)(L + L_CNT + 60);
    tg0 = __builtin_amdgcn_readfirstlane(*(LAS unsigned*)(L + L_CNT + 56)) + GOFF + w * GST;
    sfor<NPRE, NPRE + 3>([&](auto T) { constexpr int t = T, i = t - NPRE;
      bf16x8 kf[4]; kread(T, kf); score(T, kf);
      if constexpr (i > 0) sfor<(i - 1) * NPRE / 2, i * NPRE / 2>([&](auto P) { score(P, pre[P]); });
      issue(std::integral_constant<int, NF + i>{}); });
    sfor<NPRE + 3, 32>([&](auto T) { constexpr int t = T;
      if constexpr (t >= NF) wwait(T); else if constexpr (t >= NF - 3) waitvm<4 * (NF + 2 - t)>();
      bf16x8 kf[4]; kread(T, kf);
      if constexpr (t >= NF) wnext(T);
      score(T, kf);
      if constexpr (t == NF - 1) lds_arrive(L, 5);
    });
    float m = -INFINITY;
#pragma unroll
    for (int G = 0; G < 4; G++)
#pragma unroll
      for (int r = 0; r < 4; r++) { const float s = rbf(rbf(S[G][r]) * 0.08838834764831845f); S[G][r] = s; m = fmaxf(m, s); }
    m = fmaxf(m, dppf<XOR2>(m)); m = fmaxf(m, dppf<ROR4>(m)); m = fmaxf(m, dppf<ROR8>(m)); m = x16max(m); m = x32max(m);
    const float msub = (m == -INFINITY) ? 0.f : m;
    float l = 0; unsigned P[4][2];
#pragma unroll
    for (int G = 0; G < 4; G++) { float e[4];
#pragma unroll
      for (int r = 0; r < 4; r++) { e[r] = __builtin_amdgcn_exp2f((S[G][r] - msub) * 1.4426950408889634f); l += e[r]; }
      P[G][0] = pk2(e[0], e[1]); P[G][1] = pk2(e[2], e[3]); }
    l += dppf<XOR2>(l); l += dppf<ROR4>(l); l += dppf<ROR8>(l); l = x16sum(l); l = x32sum(l);
    if (lane < 2) *(LAS f32x2*)(L + L_ML + w * 16 + lane * 8) = f32x2{m, l};
    lds_arrive(L, 2);
    f32x4 acc = {0, 0, 0, 0};
    float sc; unsigned oaddr, oown; int goff;
    sfor<32, 64>([&](auto T) { constexpr int t = T; constexpr int T8 = t & 7, G = (t - 32) >> 3;
      if constexpr (t == 63) pin(acc);
      wwait(T);
      u32x2 v[8];
#pragma unroll
      for (int u = 0; u < 8; u++) v[u] = trread(ring + slot_of(t) * 16384 + vr0 + ((u << 5) ^ vx));
      wnext(T);
      const unsigned z0 = (n2 == T8) ? P[G][0] : 0u, z1 = (n2 == T8) ? P[G][1] : 0u;
      sfor<0, 8>([&](auto U) { pv1<T8, U>(acc, z0, z1, v[U]); });
      if constexpr (t == 56) { lds_wait<1>(L, 3, 1); sc = *(LAS float*)(L + L_SC + w * 8 + (n & 1) * 4);
        oaddr = L0 + OSLOT * 16384 + lane * 16; oown = oaddr + w * 4096; pin(oown); goff = WS_OG + ((kvh * 8 + b) * 2 + half) * 1024 + ((n & 1) * 128 + n2 * 16 + g * 4) * 4; pin(sc); pin(oaddr); pin(goff); }
      if constexpr (t == TW1) { if (mlp) { lds_wait<1>(L, 10, 4);
        LAS char* p = L + L_W1 + (8 * g + (n >> 2)) * 128 + (n & 3) * 8 + w * 32;
#pragma unroll
        for (int s = 0; s < 18; s++) { const u32x2 a = trread(p + s * 4096), c2 = trread(p + s * 4096 + 512); w1f[s] = u32x4{a[0], a[1], c2[0], c2[1]}; pin(w1f[s]); } } }
      if constexpr (t == TLATE) lds_arrive(L, 5);
    });
    f32x4 o0, o1, o2, o3; unsigned tk;
    asm volatile("ds_write_b128 %5, %6\n\ts_mov_b64 exec, 1\n\tds_add_rtn_u32 %4, %7, %8\n\ts_mov_b64 exec, -1\n\t"
                 "ds_read_b128 %0, %9\n\tds_read_b128 %1, %9 offset:4096\n\tds_read_b128 %2, %9 offset:8192\n\tds_read_b128 %3, %9 offset:12288\n\ts_waitcnt lgkmcnt(0)"
                 : "=&v"(o0), "=&v"(o1), "=&v"(o2), "=&v"(o3), "=&v"(tk) : "v"(oown), "v"(acc * sc), "v"(L0 + L_CNT + 4), "v"(1u), "v"(oaddr) : "memory");
    if (__builtin_amdgcn_readfirstlane(tk) == 3) {
      const f32x4 o = (o0 + o1) + (o2 + o3);
      const unsigned t10 = E & 0x3ffu;
      st128(rs, goff, u32x4{tag10(o[0], t10), tag10(o[1], t10), tag10(o[2], t10), tag10(o[3], t10)});
    }
    cfence();
  } else {
    const int sw = w - 4, ks = n >> 3, c8 = n & 7;
    if (PRIO) __builtin_amdgcn_s_setprio(PRIO);
    const unsigned Ev = __builtin_amdgcn_raw_buffer_load_b32(rs, WS_EPOCH, 0, 16);
    auto warm = [&]() {
      u64 pc; unsigned o, sz;
      asm volatile("s_getpc_b64 %0\n.Lwarm%=:\n\ts_mov_b32 %1, .Lwarm%=-fused_block\n\ts_mov_b32 %2, .Lfb_end-fused_block" : "=s"(pc), "=s"(o), "=s"(sz));
      const rsrc_t rc = mkrs((const char*)(pc - o), sz); unsigned v[8];
#pragma unroll
      for (int i = 0; i < 8; i++) v[i] = __builtin_amdgcn_raw_buffer_load_b32(rc, lane * 128 + i * 8192, 0, 0);
      asm volatile("" :: "v"(v[0]), "v"(v[1]), "v"(v[2]), "v"(v[3]), "v"(v[4]), "v"(v[5]), "v"(v[6]), "v"(v[7])); };
    if ((WARM & 1) && idx == 31 && sw == 1) warm();
    u32x4 wr[3][8];
    f32x4 a[8];
#pragma unroll
    for (int e = 0; e < 8; e++) a[e] = f32x4{0, 0, 0, 0};
    auto xpose = [&](u32x4 (&r)[8]) { u32x4 f[8];
#pragma unroll
      for (int e = 0; e < 4; e++)
#pragma unroll
        for (int j = 0; j < 4; j++) { f[2 * e][j] = lo2(r[2 * j][e], r[2 * j + 1][e]); f[2 * e + 1][j] = hi2(r[2 * j][e], r[2 * j + 1][e]); }
#pragma unroll
      for (int e = 0; e < 8; e++) r[e] = f[e]; };
    auto mm8 = [&](u32x4 af, const u32x4 (&f)[8]) {
#pragma unroll
      for (int e = 0; e < 8; e++) a[e] = mfma32(asbf8(af), asbf8(f[e]), a[e]); };
    auto redwrite = [&]() {
      if ((g >> 1) == ks) { LAS char* p = L + L_RED + ((sw * 2 + ks) * 8 + (g & 1) * 4) * 272 + c8 * 32;
#pragma unroll
        for (int r = 0; r < 4; r++) { *(LAS f32x4*)(p + r * 272) = f32x4{a[0][r], a[1][r], a[2][r], a[3][r]}; *(LAS f32x4*)(p + r * 272 + 16) = f32x4{a[4][r], a[5][r], a[6][r], a[7][r]}; } }
      lds_arrive(L, 0); };
    const int krow = 64 * sw + 32 * ks + 8 * g;
    u32x4 ew[24];
    auto early = [&]() {
      if (mlp) { const rsrc_t r1 = mkrs(w1, 576 * 9216); const int o = (8 * sw + (lane >> 3)) * 9216 + slice * 128 + (lane & 7) * 16;
#pragma unroll
        for (int i = 0; i < 18; i++) ew[i] = __builtin_amdgcn_raw_buffer_load_b128(r1, o, i * 32 * 9216, 0);
      } else { const rsrc_t ro = mkrs(wo, 4096 * 1152); int o[3];
#pragma unroll
        for (int i = 0; i < 3; i++) { const int u = 64 * i + lane; o[i] = (pk * 256 + 8 * sw + u / 24) * 1152 + cs * 384 + (u % 24) * 16; }
#pragma unroll
        for (int i = 0; i < 24; i++) ew[i] = __builtin_amdgcn_raw_buffer_load_b128(ro, o[i % 3], (i / 3) * 32 * 1152, 0);
      } };
    if (s1) {
      const rsrc_t rc = mkrs(cc, 9216), rx = mkrs(x, 9216), rm = mkrs(wm, 576 * 6912);
      const int co = (c8 * 576 + krow) * 2, wo_ = krow * 6912 + j1 * 128 + c8 * 16;
      const int fc = 32 * ks + 8 * g + 4 * (sw & 1);
      const bool isca = j1 >= 9 && j1 < 18;
      const int fb = 2 * sw + (g & 1), j24 = (g >> 1) * 16 + n;
      u32x4 cv[3], xv[3];
      cv[0] = __builtin_amdgcn_raw_buffer_load_b128(rc, co, 0, 0); cv[1] = __builtin_amdgcn_raw_buffer_load_b128(rc, co + 512, 0, 0);
      cv[2] = __builtin_amdgcn_raw_buffer_load_b128(rc, co + 1024, 0, 0);
#pragma unroll
      for (int i = 0; i < 3; i++) xv[i] = __builtin_amdgcn_raw_buffer_load_b128(rx, (isca && j24 < 24) ? fb * 1152 + (j24 + 24 * i) * 16 : 9216, 0, 0);
      const u32x2 xs = __builtin_amdgcn_raw_buffer_load_b64(rx, isca ? fb * 1152 + (j1 - 9) * 128 + n * 8 : 9216, 0, 0);
#pragma unroll
      for (int i = 0; i < 2; i++)
#pragma unroll
        for (int j = 0; j < 8; j++) wr[i][j] = __builtin_amdgcn_raw_buffer_load_b128(rm, wo_, (i * 256 + j) * 6912, 0);
      if (sw == 0) {
#pragma unroll
        for (int j = 0; j < 8; j++) wr[2][j] = __builtin_amdgcn_raw_buffer_load_b128(rm, wo_, (512 + j) * 6912, 0);
      }
      __builtin_amdgcn_sched_barrier(0); if (j1 >= 18) lds_arrive(L, 4); __builtin_amdgcn_sched_barrier(0);
      cfence();
      E = __builtin_amdgcn_readfirstlane(Ev) + 1u;
#pragma unroll
      for (int i = 0; i < 3; i++)
#pragma unroll
        for (int j = 0; j < 4; j++) { const float v0 = bf2f(cv[i][j]), v1 = hi2f(cv[i][j]);
          cv[i][j] = pk2(v0 * __builtin_amdgcn_rcpf(1.f + __expf(-v0)), v1 * __builtin_amdgcn_rcpf(1.f + __expf(-v1))); }
      if (isca) {
        float s = 0;
#pragma unroll
        for (int i = 0; i < 12; i++) s += bf2f(xv[i >> 2][i & 3]) + hi2f(xv[i >> 2][i & 3]);
        s += dppf<XOR1>(s); s += dppf<XOR2>(s); s += dppf<ROR4>(s); s += dppf<ROR8>(s); s = x32sum(s);
        const float mean = s * (1.f / 576.f); float v = 0;
#pragma unroll
        for (int i = 0; i < 12; i++) { const float d0 = bf2f(xv[i >> 2][i & 3]) - mean, d1 = hi2f(xv[i >> 2][i & 3]) - mean; v += d0 * d0; v += d1 * d1; }
        if (j24 >= 24) v = 0;
        v += dppf<XOR1>(v); v += dppf<XOR2>(v); v += dppf<ROR4>(v); v += dppf<ROR8>(v); v = x32sum(v);
        const float mb = rbf(mean), sd = rbf(sqrtf(rbf(rbf(v * (1.f / 576.f)) + 1e-5f)));
        f32x4 nx;
#pragma unroll
        for (int r = 0; r < 4; r++) nx[r] = rbf(rbf(((r & 1) ? hi2f(xs[r >> 1]) : bf2f(xs[r >> 1])) - mb) / sd);
        *(LAS f32x4*)(L + L_NX + fb * 272 + n * 16) = nx;
      }
      cfence();
      xpose(wr[0]); mm8(cv[0], wr[0]); xpose(wr[1]); mm8(cv[1], wr[1]);
      if (sw == 0) { xpose(wr[2]); mm8(cv[2], wr[2]); }
      redwrite();
      lds_wait<0>(L, 0, 4);
      const f32x4 nx = *(LAS f32x4*)(L + L_NX + c8 * 272 + fc * 4);
      f32x4 s = {0, 0, 0, 0};
#pragma unroll
      for (int p = 0; p < 8; p++) s += *(LAS f32x4*)(L + L_RED + (p * 8 + c8) * 272 + fc * 4);
      u32x4 y;
#pragma unroll
      for (int r = 0; r < 4; r++) y[r] = tag16(isca ? nx[r] * rbf(1.f + rbf(s[r])) : s[r], E);
      if (j1 < 18) { const int o = WS_HG + (sw >> 1) * 4 * HG_REP + j1 * 2048 + (sw & 1) * 1024 + lane * 16;
#pragma unroll
        for (int i = 0; i < 4; i++) st128(rs, o + i * HG_REP, y);
        lds_arrive(L, 4); }
      else if (sw < 2) st128(rs, WS_MODR + (c8 * 2304 + (j1 - 18) * 64 + fc) * 4, y);
    } else if (idx < 24 && sw < 3) {
      const rsrc_t rq = mkrs(wq, 576 * 8192);
      const int bl = 24 * xcd + idx, k3 = bl >> 6, qc = 64 * (bl & 63), k0 = 192 * k3 + krow;
#pragma unroll
      for (int j = 0; j < 8; j++) wr[0][j] = __builtin_amdgcn_raw_buffer_load_b128(rq, k0 * 8192 + qc * 2 + c8 * 16, j * 8192, 0);
      __builtin_amdgcn_sched_barrier(0); early(); __builtin_amdgcn_sched_barrier(0); lds_arrive(L, 4); __builtin_amdgcn_sched_barrier(0);
      E = __builtin_amdgcn_readfirstlane(Ev) + 1u;
      xpose(wr[0]);
#pragma unroll
      for (int e = 0; e < 8; e++) pin(wr[0][e]);
      const int hoff = WS_HG + xcd * HG_REP + (3 * k3 + sw) * 2048 + lane * 16;
      until(tstart + TH);
      u32x4 h[4];
      _Pragma("unroll 1") for (unsigned s = 0; s < SPIN_CAP; s++) {
        cfence();
        h[0] = ld128(rs, hoff); h[1] = ld128(rs, hoff + 1024); h[2] = ld128(rs, hoff + 9 * 2048); h[3] = ld128(rs, hoff + 9 * 2048 + 1024);
        if (all_ok((xor4(h[0], E) | xor4(h[1], E) | xor4(h[2], E) | xor4(h[3], E)) & 0xffffu)) break;
      }
      cfence();
      u32x4 af;
#pragma unroll
      for (int j = 0; j < 4; j++) af[j] = pk2(hi2f(h[j >> 1][(2 * j) & 3]) + hi2f(h[2 + (j >> 1)][(2 * j) & 3]), hi2f(h[j >> 1][(2 * j + 1) & 3]) + hi2f(h[2 + (j >> 1)][(2 * j + 1) & 3]));
      mm8(af, wr[0]);
      redwrite();
      lds_wait<0>(L, 0, 3);
      if (sw < 2) {
        f32x4 s = {0, 0, 0, 0};
#pragma unroll
        for (int p = 0; p < 6; p++) s += *(LAS f32x4*)(L + L_RED + (p * 8 + 4 * sw + g) * 272 + n * 16);
        u32x4 yh, yl;
#pragma unroll
        for (int r = 0; r < 4; r++) { const unsigned u = __float_as_uint(s[r]); yh[r] = (u & 0xffff0000u) | (E & 0xffffu); yl[r] = (u << 16) | (E & 0xffffu); }
        const int o = WS_QG + 2 * k3 * QG_PL + ((4 * sw + g) * 4096 + qc + 4 * n) * 4;
        st128(rs, o, yh); st128(rs, o + QG_PL, yl);
      }
    } else {
      if (idx < 24) {
        const rsrc_t rm = mkrs(wm, 576 * 6912);
        const int l24 = lane >= 24 ? 1 : 0; unsigned pf[4];
#pragma unroll
        for (int i = 0; i < 4; i++) { const int j = xcd + 8 * (2 * i + l24);
          pf[i] = __builtin_amdgcn_raw_buffer_load_b32(rm, (lane < 48 && j < 54) ? (24 * idx + lane - 24 * l24) * 6912 + j * 128 : 576 * 6912, 0, 0); }
        __builtin_amdgcn_sched_barrier(0); early(); __builtin_amdgcn_sched_barrier(0); lds_arrive(L, 4);
        asm volatile("" :: "v"(pf[0]), "v"(pf[1]), "v"(pf[2]), "v"(pf[3]));
      } else lds_arrive(L, 4);
      E = __builtin_amdgcn_readfirstlane(Ev) + 1u;
      if (bid < 8 && lane < 8) st32(rs, WS_FT + lane * 128 + bid * 4, (tstart << 16) | (E & 0xffffu));
    }
    lds_arrive(L, 9);
    if (sw == 3) {
      u32x4 qv[6]; unsigned tc;
      until(tstart + TQ);
      _Pragma("unroll 1") for (unsigned s = 0; s < SPIN_CAP; s++) {
        cfence(); unsigned bad = 0;
#pragma unroll
        for (int i = 0; i < 6; i++) qv[i] = ld128(rs, WS_QG + i * QG_PL + (b * 4096 + kvh * 256 + 4 * lane) * 4);
        tc = ld32(rs, WS_FT + xcd * 128 + (lane & 7) * 4);
#pragma unroll
        for (int i = 0; i < 6; i++) bad |= xor4(qv[i], E);
        if (all_ok(bad & 0xffffu)) break;
      }
      cfence();
      float qs[4];
#pragma unroll
      for (int r = 0; r < 4; r++) qs[r] = (__uint_as_float(hi2(qv[1][r], qv[0][r])) + __uint_as_float(hi2(qv[3][r], qv[2][r]))) + __uint_as_float(hi2(qv[5][r], qv[4][r]));
      *(LAS u32x2*)(L + L_Q + lane * 8) = u32x2{pk2(qs[0], qs[1]), pk2(qs[2], qs[3])};
      const int dt = (int)(tc - (tstart << 16)) >> 16;
      const bool tv = ((tc ^ E) & 0xffffu) == 0 && dt < 600 && dt > -600;
      float dm = tv ? (float)dt : -600.f, dn = tv ? (float)dt : 600.f;
      dm = fmaxf(dm, dppf<XOR1>(dm)); dm = fmaxf(dm, dppf<XOR2>(dm)); dm = fmaxf(dm, dppf<HMIR>(dm));
      dn = fminf(dn, dppf<XOR1>(dn)); dn = fminf(dn, dppf<XOR2>(dn)); dn = fminf(dn, dppf<HMIR>(dn));
      dm -= fminf(fmaxf(dm - dn - 60.f, 0.f), (float)GREC);
      if (lane == 0) { *(LAS unsigned*)(L + L_CNT + 60) = E; *(LAS unsigned*)(L + L_CNT + 56) = tstart + (int)dm - GL; }
      lds_arrive(L, 8);
    }
    lds_arrive(L, 2);
    lds_wait<8>(L, 5, 4); lds_wait<2>(L, 9, 4);
    if (proj) {
#pragma unroll
      for (int i = 0; i < 24; i++) *(LAS u32x4*)(L + L_WO + sw * 3072 + lane * 16 + (i / 3) * 12288 + (i % 3) * 1024) = ew[i];
      lds_arrive(L, 10);
    }
    if (mlp) {
#pragma unroll
      for (int i = 0; i < 18; i++) *(LAS u32x4*)(L + L_W1 + sw * 1024 + lane * 16 + i * 4096) = ew[i];
      lds_arrive(L, 10);
      {
        const int l48 = lane < 48 ? lane : 47, bb = grp * 4 + sw;
        u32x4 sbv[3], cbv[3], gav, gbv;
        _Pragma("unroll 1") for (unsigned s = 0; s < SPIN_CAP; s++) {
          cfence(); unsigned bad = 0;
#pragma unroll
          for (int i = 0; i < 3; i++) { sbv[i] = ld128(rs, WS_MODR + (bb * 2304 + 576 + 192 * i + 4 * l48) * 4); cbv[i] = ld128(rs, WS_MODR + (bb * 2304 + 1152 + 192 * i + 4 * l48) * 4); }
          gav = ld128(rs, WS_MODR + (tb * 2304 + tcol) * 4); gbv = ld128(rs, WS_MODR + (tb * 2304 + 1728 + tcol) * 4);
#pragma unroll
          for (int i = 0; i < 3; i++) bad |= xor4(sbv[i], E) | xor4(cbv[i], E);
          if (all_ok((bad | xor4(gav, E) | xor4(gbv, E)) & 0xffffu)) break;
        }
        cfence();
#pragma unroll
        for (int i = 0; i < 6; i++) { g1p[i] = pk2(1.f + hi2f(cbv[i >> 1][(2 * i) & 3]), 1.f + hi2f(cbv[i >> 1][(2 * i + 1) & 3])); sbp[i] = hi2(sbv[i >> 1][(2 * i) & 3], sbv[i >> 1][(2 * i + 1) & 3]); pin(g1p[i]); pin(sbp[i]); }
        if (sw == 3 && lane < 8) { const u32x2 xr = *(const u32x2*)(x + (tb * 576 + tcol) * 2);
          *(LAS u32x4*)(L + L_TP + lane * 48) = gav & 0xffff0000u; *(LAS u32x4*)(L + L_TP + lane * 48 + 16) = gbv & 0xffff0000u;
          *(LAS u32x4*)(L + L_TP + lane * 48 + 32) = u32x4{xr[0] << 16, xr[0] & 0xffff0000u, xr[1] << 16, xr[1] & 0xffff0000u}; }
      }
      lds_arrive(L, 9);
    }
    const char* w2b = w2 + (size_t)slice * 73728;
    if (mlp && sw > 0) { _Pragma("unroll 1") for (int gi = sw - 1; gi < NE2; gi += 3) { dma1<WNT>(w2b + gi * 1024, lane * 16, L0 + L_W2 + gi * 1024); waitvm<W1W>(); } }
    if (sw == 0) {
      lds_wait<8>(L, 2, 8);
      const int hh = lane & 1, ww = (lane >> 1) & 3;
      float mw[4], lw[4];
#pragma unroll
      for (int i = 0; i < 4; i++) { const f32x2 v = *(LAS f32x2*)(L + L_ML + i * 16 + hh * 8); mw[i] = v[0]; lw[i] = v[1]; }
      const float Mc = fmaxf(fmaxf(mw[0], mw[1]), fmaxf(mw[2], mw[3])), Ms = (Mc == -INFINITY) ? 0.f : Mc;
      float Lc = 0;
#pragma unroll
      for (int i = 0; i < 4; i++) Lc += lw[i] * __builtin_amdgcn_exp2f((mw[i] - Ms) * 1.4426950408889634f);
      const unsigned tm = tag16(Mc, E), tl = tag10(Lc, E & 0x3ffu);
      const u32x4 rec = {dppu<0x00>(tm), dppu<0x55>(tm), dppu<0x00>(tl), dppu<0x55>(tl)};
      if (lane == 0) st128(rs, WS_ML + bid * 128, rec);
      u32x4 o;
      _Pragma("unroll 1") for (unsigned s = 0; s < SPIN_CAP; s++) {
        cfence();
        o = ld128(rs, WS_ML + (bid ^ 128) * 128);
        if (all_ok((((o[0] ^ E) | (o[1] ^ E)) & 0xffffu) | (((o[2] ^ E) | (o[3] ^ E)) & 0x3ffu))) break;
        __builtin_amdgcn_s_sleep(8);
      }
      cfence();
      const float Mo = hi2f(hh ? o[1] : o[0]), Lo = un10(hh ? o[3] : o[2]);
      const float M = fmaxf(Mc, Mo);
      const float Lt = un10(tl) * __builtin_amdgcn_exp2f((Mc - M) * 1.4426950408889634f) + Lo * __builtin_amdgcn_exp2f((Mo - M) * 1.4426950408889634f);
      const float mine = *(LAS float*)(L + L_ML + ww * 16 + hh * 8);
      if (lane < 8) *(LAS float*)(L + L_SC + lane * 4) = __builtin_amdgcn_exp2f((mine - M) * 1.4426950408889634f) / Lt;
      lds_arrive(L, 3);
    }
    if ((WARM & 2) && idx == 31 && sw == 1) { until(tstart + TWARM); warm(); }
    if (mlp) {
      lds_wait<1>(L, 5, 8);
      _Pragma("unroll 1") for (int gi = NE2 + sw; gi < 72; gi += 4) { dma1<WNT>(w2b + gi * 1024, lane * 16, L0 + L_W2 + gi * 1024); waitvm<WL2>(); }
      waitvm<0>();
      lds_arrive(L, 10);
    }
    if (PRIO && mlp) __builtin_amdgcn_s_setprio(0);
  }
  const int n4 = n >> 2;
  const unsigned tend = __builtin_amdgcn_readfirstlane(*(volatile LAS unsigned*)(L + L_CNT + 56)) + GOFF + 63 * GTAU / 10 + 3 * GST + TEND;
  unsigned E10 = E & 0x3ffu;
  if (proj) {
    const int pw = w ^ 4;
    if (pw < 6) {
      lds_wait<1>(L, 10, 4);
      LAS char* p = L + L_WO + (8 * g + (n >> 2)) * 384 + (n & 3) * 8 + pw * 64;
#pragma unroll
      for (int s = 0; s < 8; s++)
#pragma unroll
        for (int u = 0; u < 2; u++) { const u32x2 a = trread(p + s * 12288 + u * 32), c2 = trread(p + s * 12288 + u * 32 + 1536); wof[u][s] = u32x4{a[0], a[1], c2[0], c2[1]}; pin(wof[u][s]); }
    }
    int p1off = WS_P1 + (((n & 7) * 16 + pk) * 576 + cs * 192 + pw * 32 + (n >> 3) * 16 + 4 * g) * 4;
    unsigned ob = L_OB + (n & 7) * OB_STR + g * 16;
    pin(p1off); pin(ob); pin(E10);
    if (w >= 4) {
      int go = WS_OG + (pk * 8 + 2 * (w - 4)) * 2048 + lane * 16; unsigned ow = L_OB + 2 * (w - 4) * OB_STR + lane * 8;
      pin(go); pin(ow);
      until(tend + FO); lds_wait<0>(L, 1, 4); __builtin_amdgcn_s_sleep(SO);
      u32x4 p[4];
      _Pragma("unroll 1") for (unsigned s = 0; s < SPIN_CAP; s++) {
        cfence(); unsigned bad = 0;
#pragma unroll
        for (int i = 0; i < 4; i++) p[i] = __builtin_amdgcn_raw_buffer_load_b128(rs, go, i * 1024, 16);
#pragma unroll
        for (int i = 0; i < 4; i++) bad |= xor4(p[i], E);
        if (all_ok(bad & 0x3ffu)) break;
      }
      cfence();
#pragma unroll
      for (int i = 0; i < 2; i++) *(LAS u32x2*)(L + ow + i * OB_STR) = u32x2{pk2(un10(p[2 * i][0]) + un10(p[2 * i + 1][0]), un10(p[2 * i][1]) + un10(p[2 * i + 1][1])),
                                                                             pk2(un10(p[2 * i][2]) + un10(p[2 * i + 1][2]), un10(p[2 * i][3]) + un10(p[2 * i + 1][3]))};
    }
    wg_barrier();
    if (pw < 6) {
      f32x4 d0 = {0, 0, 0, 0}, d1 = {0, 0, 0, 0}; bf16x8 of[8];
#pragma unroll
      for (int s = 0; s < 8; s++) of[s] = *(LAS bf16x8*)(L + ob + s * 64);
#pragma unroll
      for (int s = 0; s < 8; s++) { d0 = mfma32(asbf8(wof[0][s]), of[s], d0); d1 = mfma32(asbf8(wof[1][s]), of[s], d1); }
      u32x4 y;
#pragma unroll
      for (int r = 0; r < 4; r++) y[r] = tag10(n < 8 ? d0[r] : d1[r], E10);
      st128(rs, p1off, y);
    }
    cfence();
  }
  if (mlp) {
    u32x4 w2a[5], w2b[5];
    auto w2read = [&]() {
      LAS char* pa = L + L_W2 + (8 * g + (n >> 2)) * 1152 + (n & 3) * 8;
      LAS char* pb = L + L_W2 + (32 + 8 * g + (n >> 2)) * 1152 + (n & 3) * 8;
#pragma unroll
      for (int u = 0; u < 5; u++) { const int o = u < 4 ? w * 128 + u * 32 : 1024 + (w & 3) * 32;
        const u32x2 a = trread(pa + o), c2 = trread(pa + o + 4608); w2a[u] = u32x4{a[0], a[1], c2[0], c2[1]};
        const u32x2 a2 = trread(pb + o), c3 = trread(pb + o + 4608); w2b[u] = u32x4{a2[0], a2[1], c3[0], c3[1]}; } };
    f32x4 x1v, gbv;
    auto w2get = [&]() { lds_wait<1>(L, 10, 8); w2read();
#pragma unroll
      for (int u = 0; u < 5; u++) { pin(w2a[u]); pin(w2b[u]); } };
    if (w != 3) w2get();
    if (w == 3) {
      lds_wait<1>(L, 9, 8); lds_wait<1>(L, 1, 4);
      {
        const f32x4 gav = *(LAS f32x4*)(L + L_TP + (lane & 7) * 48), xv = *(LAS f32x4*)(L + L_TP + (lane & 7) * 48 + 32);
        gbv = *(LAS f32x4*)(L + L_TP + (lane & 7) * 48 + 16);
        int po = WS_P1 + ((tb * 16 + p8) * 576 + tcol) * 4, xo = WS_X1 + p8 * X1_REP + (tb * 576 + tcol) * 4;
        pin(po); pin(xo);
        __builtin_amdgcn_s_sleep(SB); until(tend + FB);
        u32x4 p[2];
        _Pragma("unroll 1") for (unsigned s = 0; s < SPIN_CAP; s++) {
          cfence();
          p[0] = ld128(rs, po); p[1] = __builtin_amdgcn_raw_buffer_load_b128(rs, po, 8 * 2304, 16);
          if (all_ok((xor4(p[0], E) | xor4(p[1], E)) & 0x3ffu)) break;
          }
        lds_arrive(L, 11);
        u32x4 y;
#pragma unroll
        for (int r = 0; r < 4; r++) {
          float s = un10(p[0][r]) + un10(p[1][r]);
          s += dppf<ROR8>(s); s = x16sum(s); s = x32sum(s);
          y[r] = tag16(xv[r] + rbf(gav[r] * rbf(s)), E); x1v[r] = hi2f(y[r]);
        }
        st128(rs, xo, y);
        cfence();
      }
      w2get();
    } else if (w >= 4) {
      int xo = WS_X1 + xcd * X1_REP + ((grp * 4 + w - 4) * 576 + 4 * (lane < 48 ? lane : 47)) * 4;
      unsigned ha = L_H2 + (w - 4) * H2_STR + lane * 8;
      float g1[12], sb[12];
#pragma unroll
      for (int i = 0; i < 12; i++) { g1[i] = (i & 1) ? hi2f(g1p[i >> 1]) : bf2f(g1p[i >> 1]); sb[i] = (i & 1) ? hi2f(sbp[i >> 1]) : bf2f(sbp[i >> 1]); pin(g1[i]); pin(sb[i]); }
      pin(xo); pin(ha);
      until(tend + FX); lds_wait<0>(L, 11, 1); __builtin_amdgcn_s_sleep(SX);
      u32x4 xv[3];
      _Pragma("unroll 1") for (unsigned s = 0; s < SPIN_CAP; s++) {
        cfence(); unsigned bad = 0;
#pragma unroll
        for (int i = 0; i < 3; i++) xv[i] = ld128(rs, xo + 768 * i);
#pragma unroll
        for (int i = 0; i < 3; i++) bad |= xor4(xv[i], E);
        if (all_ok(bad & 0xffffu)) break;
      }
      cfence();
      const float x0 = hi2f(__builtin_amdgcn_readfirstlane(xv[0][0]));
      float xf[12], s1 = 0, s2 = 0;
#pragma unroll
      for (int i = 0; i < 12; i++) { xf[i] = hi2f(xv[i >> 2][i & 3]); const float dd = xf[i] - x0; s1 += dd; s2 += dd * dd; }
      if (lane >= 48) { s1 = 0; s2 = 0; }
      s1 += dppf<XOR1>(s1); s2 += dppf<XOR1>(s2); s1 += dppf<XOR2>(s1); s2 += dppf<XOR2>(s2);
      s1 += dppf<ROR4>(s1); s2 += dppf<ROR4>(s2); s1 += dppf<ROR8>(s1); s2 += dppf<ROR8>(s2);
      s1 = x16sum(s1); s2 = x16sum(s2); s1 = x32sum(s1); s2 = x32sum(s2);
      const float md = s1 * (1.f / 576.f), var = fmaxf(s2 * (1.f / 576.f) - md * md, 0.f);
      const float mb = rbf(x0 + md), rs_ = __builtin_amdgcn_rcpf(rbf(__builtin_amdgcn_sqrtf(rbf(rbf(var) + 1e-5f))));
      if (lane < 48) {
#pragma unroll
        for (int i = 0; i < 3; i++) { float hh[4];
#pragma unroll
          for (int j = 0; j < 4; j++) hh[j] = (xf[4 * i + j] - mb) * rs_ * g1[4 * i + j] + sb[4 * i + j];
          *(LAS u32x2*)(L + ha + 384 * i) = u32x2{pk2(hh[0], hh[1]), pk2(hh[2], hh[3])}; }
      }
    }
    int po = WS_P2 + (((grp * 4 + (n & 3)) * 72 + slice) * 576 + 4 * g) * 4, po2 = n < 4 ? po + (448 + 16 * w) * 4 : WS_END;
    po += (64 * w + 16 * n4) * 4;
    int fo = WS_P2 + ((tb * 72 + p8) * 576 + tcol) * 4;
    unsigned h1a = L_H1 + (n & 3) * H1_STR + g * 16, h2a = L_H2 + (n & 3) * H2_STR + g * 16, h1w = L_H1 + n * H1_STR + (w * 16 + 4 * g) * 2;
    pin(po); pin(po2); pin(h1a); pin(h2a); pin(h1w); pin(fo);
    wg_barrier();
    if (w < 4) {
      f32x4 d = {0, 0, 0, 0}; bf16x8 hf[18];
#pragma unroll
      for (int s = 0; s < 18; s++) hf[s] = *(LAS bf16x8*)(L + h2a + s * 64);
#pragma unroll
      for (int s = 0; s < 18; s++) d = mfma32(asbf8(w1f[s]), hf[s], d);
#pragma unroll
      for (int r = 0; r < 4; r++) d[r] = d[r] < 0.f ? 0.f : d[r];
      if (n < 4) *(LAS u32x2*)(L + h1w) = u32x2{pk2(d[0], d[1]), pk2(d[2], d[3])};
    }
    wg_barrier();
    {
      const bf16x8 ha = *(LAS bf16x8*)(L + h1a), hb = *(LAS bf16x8*)(L + h1a + 64);
      f32x4 d[5];
#pragma unroll
      for (int u = 0; u < 4; u++) d[u] = mfma32(asbf8(w2b[u]), hb, mfma32(asbf8(w2a[u]), ha, f32x4{0, 0, 0, 0}));
      if (w >= 4) d[4] = mfma32(asbf8(w2b[4]), hb, mfma32(asbf8(w2a[4]), ha, f32x4{0, 0, 0, 0}));
      u32x4 y;
#pragma unroll
      for (int r = 0; r < 4; r++) y[r] = tag10(sel4(d[0][r], d[1][r], d[2][r], d[3][r]), E10);
      st128(rs, po, y);
      if (w >= 4) st128(rs, po2, u32x4{tag10(d[4][0], E10), tag10(d[4][1], E10), tag10(d[4][2], E10), tag10(d[4][3], E10)});
    }
    cfence();
    if (w == 3) {
      __builtin_amdgcn_s_sleep(SC); if (FC) until(tend + FC);
      u32x4 p[9];
      _Pragma("unroll 1") for (unsigned s = 0; s < SPIN_CAP; s++) {
        cfence(); unsigned bad = 0;
#pragma unroll
        for (int i = 0; i < 9; i++) p[i] = __builtin_amdgcn_raw_buffer_load_b128(rs, fo, i * 8 * 2304, 16);
#pragma unroll
        for (int i = 0; i < 9; i++) bad |= xor4(p[i], E);
        if (all_ok(bad & 0x3ffu)) break;
      }
      cfence();
      float o[4];
#pragma unroll
      for (int r = 0; r < 4; r++) { float t = 0;
#pragma unroll
        for (int i = 0; i < 3; i++) t += (un10(p[3 * i][r]) + un10(p[3 * i + 1][r])) + un10(p[3 * i + 2][r]);
        t += dppf<ROR8>(t); t = x16sum(t); t = x32sum(t); o[r] = x1v[r] + rbf(gbv[r] * rbf(t)); }
      if (lane < 8) *(u32x2*)(out + (tb * 576 + tcol) * 2) = u32x2{pk2(o[0], o[1]), pk2(o[2], o[3])};
    }
  } else if (bid == 248 && w == 1) {
    _Pragma("unroll 1") for (unsigned s = 0; s < SPIN_CAP; s++) {
      cfence();
      const u32x4 v = ld128(rs, WS_X1 + (lane & 7) * 576 * 4);
      if (all_ok(xor4(v, E) & 0xffffu)) break;
      __builtin_amdgcn_s_sleep(4);
    }
    cfence();
    if (lane == 0) st32(rs, WS_EPOCH, E);
  }
  asm volatile(".Lfb_end:");
}

extern "C" void kernel_launch(void* const* d_in, const int* in_sizes, int n_in, void* d_out, int out_size, void* d_ws, size_t ws_size, hipStream_t stream) {
  fused_block<<<256, 512, 0, stream>>>((const char*)d_in[1], (const char*)d_in[2], (const char*)d_in[0], (const char*)d_in[3], (const char*)d_in[4], (const char*)d_in[6],
                                       (const char*)d_in[7], (const char*)d_in[8], (const char*)d_in[9], (char*)d_out, (char*)d_ws);
}
```
